```python
import torch

def _softmax(x, axis=-1):
    return torch.softmax(x, dim=axis)
def _gelu(x):
    return torch.nn.functional.gelu(x, approximate="tanh")
def _silu(x):
    return torch.nn.functional.silu(x)
def _sigmoid(x):
    return torch.sigmoid(x)
def _layernorm(x, axis=-1, eps=1e-5):
    m = x.mean(dim=axis, keepdim=True)
    v = x.var(dim=axis, keepdim=True, unbiased=False)
    return (x - m) / torch.sqrt(v + eps)
def _scatter_add(tgt, idx, src, axis=0):
    return tgt.clone().index_add_(axis, idx, src)
def _segment_sum(src, seg, n):
    out = torch.zeros((n,) + src.shape[1:], dtype=src.dtype)
    return out.index_add_(0, seg, src)
def _pad(x, pw):
    flat = tuple(v for pair in reversed(pw) for v in pair)
    return torch.nn.functional.pad(x, flat)

kernel_name = 'sequential_preLN__gqa_block_cached__geglu_ffn__decode'

def setup_inputs():

    _w = (lambda a: (lambda t: t.to(torch.bfloat16) if t.is_floating_point() else t)(torch.as_tensor(a)))
    def _A_setup():
        B, Sq, D = 4, 1, 512
        Hq, Hkv, Sk, Dh = 32, 32, 8192, 128
        assert Hq % Hkv == 0, (Hq, Hkv)
        return {
            "x": _w((lambda *s: torch.randn(*s, dtype=torch.bfloat16) * 0.05)(B, Sq, D)),
            "k_cache": _w((lambda *s: torch.randn(*s, dtype=torch.bfloat16) * 3.2)(B, Hkv, Sk, Dh)),
            "v_cache": _w((lambda *s: torch.randn(*s, dtype=torch.bfloat16) * 0.4)(B, Hkv, Sk, Dh)),
            "wq": _w((lambda *s: torch.randn(*s, dtype=torch.bfloat16) * 3.2)(D, Hq * Dh) * (D ** -0.5)),
            "wo": _w((lambda *s: torch.randn(*s, dtype=torch.bfloat16) * 0.4)(Hq * Dh, D) * ((Hq * Dh) ** -0.5)),
        }
    _A_in = _A_setup()

    def _B_setup():
        B, S, D, F = 4, 1, 512, 1376
        return {
            "x": _w((lambda *s: torch.randn(*s, dtype=torch.bfloat16) * 0.05)(B, S, D)),
            "w_gate": _w((lambda *s: torch.randn(*s, dtype=torch.bfloat16) * 0.05)(D, F)),
            "w_up": _w((lambda *s: torch.randn(*s, dtype=torch.bfloat16) * 0.05)(D, F)),
            "w_down": _w((lambda *s: torch.randn(*s, dtype=torch.bfloat16) * 0.05)(F, D)),
        }
    _B_in = _B_setup()

    return {
        "A_x": _A_in["x"],
        "A_k_cache": _A_in["k_cache"],
        "A_v_cache": _A_in["v_cache"],
        "A_wq": _A_in["wq"],
        "A_wo": _A_in["wo"],
        "B_x": _B_in["x"],
        "B_w_gate": _B_in["w_gate"],
        "B_w_up": _B_in["w_up"],
        "B_w_down": _B_in["w_down"],
    }

def reference(A_x, A_k_cache, A_v_cache, A_wq, A_wo, B_x, B_w_gate, B_w_up, B_w_down):
    def _A_ref(x, k_cache, v_cache, wq, wo):
        B, Sq, D = x.shape
        Hq, Dh = 32, 128
        Hkv = k_cache.shape[1]
        rep = Hq // Hkv
        q = (lambda x, *d: x.permute(*d))((x @ wq).reshape(B, Sq, Hq, Dh), 0, 2, 1, 3)
        k = (lambda x, n, axis: torch.repeat_interleave(x, n, dim=axis))(k_cache, rep, axis=1)
        v = (lambda x, n, axis: torch.repeat_interleave(x, n, dim=axis))(v_cache, rep, axis=1)
        scores = torch.einsum("bhqd,bhkd->bhqk", q, k) * (Dh ** -0.5)
        p = _softmax(scores, axis=-1)
        o = torch.einsum("bhqk,bhkd->bhqd", p, v)
        return (lambda x, *d: x.permute(*d))(o, 0, 2, 1, 3).reshape(B, Sq, Hq * Dh) @ wo

    def _B_ref(x, w_gate, w_up, w_down):
        return (_gelu(x @ w_gate) * (x @ w_up)) @ w_down

    x = A_x
    h = x + _A_ref(_layernorm(x, axis=-1), A_k_cache, A_v_cache, A_wq, A_wo)
    h2 = h + _B_ref(_layernorm(h, axis=-1), B_w_gate, B_w_up, B_w_down)
    return h2
```

```cpp
#include <hip/hip_runtime.h>
#include <hip/hip_bf16.h>
#include <stdint.h>

#define MIMIC 0

constexpr int B = 4, D = 512, H = 32, SK = 8192, DH = 128, F = 1376;
constexpr int NW = 8, NTHR = NW * 64;
constexpr int NSPLIT = 2;
constexpr int KEYS_WG = SK / NSPLIT;
constexpr int TK = 32;
constexpr int NL = TK / 4;
constexpr int TILES_W = KEYS_WG / (NW * TK);

typedef unsigned u32x4 __attribute__((ext_vector_type(4)));
typedef float f32x4 __attribute__((ext_vector_type(4)));
typedef __bf16 bf16x2 __attribute__((ext_vector_type(2)));

__device__ __forceinline__ float dot2(unsigned k, unsigned q, float acc) {
  return __builtin_amdgcn_fdot2_f32_bf16(__builtin_bit_cast(bf16x2, k), __builtin_bit_cast(bf16x2, q), acc, false);
}
__device__ __forceinline__ float bflo(unsigned u) { return __builtin_bit_cast(float, u << 16); }
__device__ __forceinline__ float bfhi(unsigned u) { return __builtin_bit_cast(float, u & 0xffff0000u); }
__device__ __forceinline__ unsigned cvtpk(float lo, float hi) {
  unsigned r; asm("v_cvt_pk_bf16_f32 %0, %1, %2" : "=v"(r) : "v"(lo), "v"(hi)); return r;
}
__device__ __forceinline__ float rbf(float x) { return bflo(cvtpk(x, x)); }
template <int CTRL> __device__ __forceinline__ float dpp(float x) {
  return __builtin_bit_cast(float, __builtin_amdgcn_mov_dpp(__builtin_bit_cast(int, x), CTRL, 0xf, 0xf, true));
}
constexpr int XOR1 = 0xB1, XOR2 = 0x4E, XOR7 = 0x141, XOR8 = 0x128, ROR4 = 0x124, ROR2 = 0x122, ROR1 = 0x121;
constexpr int BCAST0 = 0x150;
__device__ __forceinline__ float xrow_max(float x) {
  auto s = __builtin_amdgcn_permlane16_swap(__float_as_uint(x), __float_as_uint(x), false, false);
  x = fmaxf(__uint_as_float(s[0]), __uint_as_float(s[1]));
  auto t = __builtin_amdgcn_permlane32_swap(__float_as_uint(x), __float_as_uint(x), false, false);
  return fmaxf(__uint_as_float(t[0]), __uint_as_float(t[1]));
}
__device__ __forceinline__ float xrow_sum(float x) {
  auto s = __builtin_amdgcn_permlane16_swap(__float_as_uint(x), __float_as_uint(x), false, false);
  x = __uint_as_float(s[0]) + __uint_as_float(s[1]);
  auto t = __builtin_amdgcn_permlane32_swap(__float_as_uint(x), __float_as_uint(x), false, false);
  return __uint_as_float(t[0]) + __uint_as_float(t[1]);
}
__device__ __forceinline__ float row_sum16(float x) {
  x += dpp<XOR8>(x); x += dpp<XOR7>(x); x += dpp<XOR2>(x); x += dpp<XOR1>(x); return x;
}
__device__ __forceinline__ float row_max16(float x) {
  x = fmaxf(x, dpp<XOR8>(x)); x = fmaxf(x, dpp<XOR7>(x)); x = fmaxf(x, dpp<XOR2>(x)); x = fmaxf(x, dpp<XOR1>(x)); return x;
}
__device__ __forceinline__ float wave_sum(float x) { return xrow_sum(row_sum16(x)); }

typedef __amdgpu_buffer_rsrc_t rsrc_t;
__device__ __forceinline__ rsrc_t mkrsrc(const void* p, unsigned bytes) {
  return __builtin_amdgcn_make_buffer_rsrc((void*)p, 0, bytes, 0x00020000);
}
constexpr int AUX_NT = 2;
template <int AUX> __device__ __forceinline__ u32x4 bload(rsrc_t r, int voff, int soff) {
  return __builtin_amdgcn_raw_buffer_load_b128(r, voff, soff, AUX);
}

template <int I> struct IC { static constexpr int v = I; };

__device__ __forceinline__ float qk_softmax(const u32x4 (&K)[NL], const u32x4& q, int c, float& m, float& l, float& alpha) {
  constexpr float C2 = 0.088388347648318440f * 1.4426950408889634f;
  float x[NL];
#pragma unroll
  for (int i = 0; i < NL; ++i) {
    float d = dot2(K[i][0], q[0], 0.f);
    d = dot2(K[i][1], q[1], d); d = dot2(K[i][2], q[2], d); d = dot2(K[i][3], q[3], d);
    x[i] = d;
  }
#pragma unroll
  for (int h = 0; h < 4; ++h) { const float t1 = x[h] + dpp<XOR8>(x[h]), t2 = x[h + 4] + dpp<XOR8>(x[h + 4]); x[h] = (c & 8) ? t2 : t1; }
#pragma unroll
  for (int h = 0; h < 2; ++h) { const float t1 = x[h] + dpp<XOR7>(x[h]), t2 = x[h + 2] + dpp<XOR7>(x[h + 2]); x[h] = (c & 4) ? t2 : t1; }
  { const float t1 = x[0] + dpp<XOR2>(x[0]), t2 = x[1] + dpp<XOR2>(x[1]); x[0] = (c & 2) ? t2 : t1; }
  float s = x[0] + dpp<XOR1>(x[0]);
  s = s * C2;
  const float mn = fmaxf(m, row_max16(s));
  alpha = __builtin_amdgcn_exp2f(m - mn);
  const float p = __builtin_amdgcn_exp2f(s - mn);
  l = fmaf(l, alpha, p);
  m = mn;
  return p;
}
template <int I> __device__ __forceinline__ void fma_row(const u32x4& V, float p, float (&acc)[8]) {
  const float pi = dpp<BCAST0 + I>(p);
#pragma unroll
  for (int k = 0; k < 4; ++k) { acc[2 * k] = fmaf(pi, bflo(V[k]), acc[2 * k]); acc[2 * k + 1] = fmaf(pi, bfhi(V[k]), acc[2 * k + 1]); }
}
__device__ __forceinline__ void pv(const u32x4 (&V)[NL], float p, float alpha, float (&acc)[8]) {
#pragma unroll
  for (int k = 0; k < 8; ++k) acc[k] *= alpha;
  fma_row<0>(V[0], p, acc); fma_row<2>(V[1], p, acc); fma_row<4>(V[2], p, acc); fma_row<6>(V[3], p, acc);
  fma_row<8>(V[4], p, acc); fma_row<10>(V[5], p, acc); fma_row<12>(V[6], p, acc); fma_row<14>(V[7], p, acc);
}
template <int AUX> __device__ __forceinline__ void issue(u32x4 (&T)[NL], rsrc_t r, int voff, int soff) {
#pragma unroll
  for (int i = 0; i < NL; ++i) T[i] = bload<AUX>(r, voff + i * 1024, soff);
  __builtin_amdgcn_sched_barrier(0);
}

struct Params {
  const uint16_t* x; const uint16_t* k; const uint16_t* v; const uint16_t* wq; const uint16_t* wo;
  const uint16_t* wg; const uint16_t* wu; const uint16_t* wd; uint16_t* out; float* ws;
};
constexpr int PART_REC = 132;

__global__ void __launch_bounds__(NTHR, 2) attn_kernel(Params p) {
  __shared__ __attribute__((aligned(16))) float lds[NW * PART_REC];
  const int tid = threadIdx.x, lane = tid & 63, w = __builtin_amdgcn_readfirstlane(tid >> 6), r = lane >> 4, c = lane & 15;
  const int blk = blockIdx.x;
  const int h = (blk & 7) | ((blk >> 6) << 3), b = (blk >> 3) & 3, s = (blk >> 5) & 1;
  const size_t kvoff = ((size_t)(b * H + h) * SK + (size_t)s * KEYS_WG) * DH;
  const rsrc_t rk = mkrsrc(p.k + kvoff, KEYS_WG * DH * 2), rv = mkrsrc(p.v + kvoff, KEYS_WG * DH * 2);
  const rsrc_t rq = mkrsrc(p.wq + h * DH, D * H * DH * 2 - h * DH * 2);
  const u32x4 xv = *reinterpret_cast<const u32x4*>(p.x + b * D + lane * 8);
  const unsigned xo = p.x[b * D + 64 * w + 4 * c + r];
  u32x4 K0[NL], V0[NL], K1[NL], V1[NL];
  {
    const int voff = r * 8192 + c * 16, soff = w * 64 * 8192;
#pragma unroll
    for (int i = 0; i < NL; ++i) K0[i] = bload<0>(rq, voff + i * 4 * 8192, soff);
#pragma unroll
    for (int i = 0; i < NL; ++i) V0[i] = bload<0>(rq, voff + (i + 8) * 4 * 8192, soff);
    __builtin_amdgcn_sched_barrier(0);
  }
  const int voff = lane * 16;
  const int tstride = NW * TK * 256;
  int soff = w * TK * 256;
  issue<AUX_NT>(K1, rk, voff, soff); issue<AUX_NT>(V1, rv, voff, soff);
  float xn;
  {
    float xs[8];
#pragma unroll
    for (int k = 0; k < 4; ++k) { xs[2 * k] = bflo(xv[k]); xs[2 * k + 1] = bfhi(xv[k]); }
    float sum = 0.f;
#pragma unroll
    for (int k = 0; k < 8; ++k) sum += xs[k];
    const float mean = wave_sum(sum) * (1.f / D);
    float sq = 0.f;
#pragma unroll
    for (int k = 0; k < 8; ++k) { const float d = xs[k] - mean; sq = fmaf(d, d, sq); }
    const float var = wave_sum(sq) * (1.f / D);
    const float xe = bflo(xo);
    xn = rbf((xe - mean) / sqrtf(var + 1e-5f));
  }
  float acc[8];
#pragma unroll
  for (int k = 0; k < 8; ++k) acc[k] = 0.f;
  fma_row<0>(K0[0], xn, acc); fma_row<1>(K0[1], xn, acc); fma_row<2>(K0[2], xn, acc); fma_row<3>(K0[3], xn, acc);
  fma_row<4>(K0[4], xn, acc); fma_row<5>(K0[5], xn, acc); fma_row<6>(K0[6], xn, acc); fma_row<7>(K0[7], xn, acc);
  __builtin_amdgcn_sched_barrier(0);
  issue<AUX_NT>(K0, rk, voff, soff + tstride);
  fma_row<8>(V0[0], xn, acc); fma_row<9>(V0[1], xn, acc); fma_row<10>(V0[2], xn, acc); fma_row<11>(V0[3], xn, acc);
  fma_row<12>(V0[4], xn, acc); fma_row<13>(V0[5], xn, acc); fma_row<14>(V0[6], xn, acc); fma_row<15>(V0[7], xn, acc);
  __builtin_amdgcn_sched_barrier(0);
  issue<AUX_NT>(V0, rv, voff, soff + tstride);
#pragma unroll
  for (int k = 0; k < 8; ++k) acc[k] = xrow_sum(acc[k]);
  if (r == 0) {
    *reinterpret_cast<f32x4*>(&lds[w * DH + 8 * c]) = f32x4{acc[0], acc[1], acc[2], acc[3]};
    *reinterpret_cast<f32x4*>(&lds[w * DH + 8 * c + 4]) = f32x4{acc[4], acc[5], acc[6], acc[7]};
  }
  asm volatile("s_waitcnt lgkmcnt(0)" ::: "memory");
  __builtin_amdgcn_s_barrier();
  u32x4 q;
  {
    f32x4 a = {0, 0, 0, 0}, bq = {0, 0, 0, 0};
#pragma unroll
    for (int ww = 0; ww < NW; ++ww) {
      a += *reinterpret_cast<const f32x4*>(&lds[ww * DH + 8 * c]);
      bq += *reinterpret_cast<const f32x4*>(&lds[ww * DH + 8 * c + 4]);
    }
    q[0] = cvtpk(a[0], a[1]); q[1] = cvtpk(a[2], a[3]); q[2] = cvtpk(bq[0], bq[1]); q[3] = cvtpk(bq[2], bq[3]);
  }
  asm volatile("s_waitcnt lgkmcnt(0)" ::: "memory");
  __builtin_amdgcn_s_barrier();
  float m = -INFINITY, l = 0.f, alpha;
#pragma unroll
  for (int k = 0; k < 8; ++k) acc[k] = 0.f;
  soff += 2 * tstride;
#pragma unroll 1
  for (int t = 0; t < TILES_W; t += 2) {
    float pp = qk_softmax(K1, q, c, m, l, alpha);
    __builtin_amdgcn_sched_barrier(0);
    issue<AUX_NT>(K1, rk, voff, soff);
    pv(V1, pp, alpha, acc);
    __builtin_amdgcn_sched_barrier(0);
    issue<AUX_NT>(V1, rv, voff, soff);
    pp = qk_softmax(K0, q, c, m, l, alpha);
    __builtin_amdgcn_sched_barrier(0);
    issue<AUX_NT>(K0, rk, voff, soff + tstride);
    pv(V0, pp, alpha, acc);
    __builtin_amdgcn_sched_barrier(0);
    issue<AUX_NT>(V0, rv, voff, soff + tstride);
    soff += 2 * tstride;
  }
  l = row_sum16(l) * 0.5f;
  const float mw = xrow_max(m);
  const float f = __builtin_amdgcn_exp2f(m - mw);
  l = xrow_sum(l * f);
#pragma unroll
  for (int k = 0; k < 8; ++k) acc[k] = xrow_sum(acc[k] * f);
  if (r == 0) {
    float* rec = &lds[w * PART_REC];
    *reinterpret_cast<f32x4*>(&rec[4 + 8 * c]) = f32x4{acc[0], acc[1], acc[2], acc[3]};
    *reinterpret_cast<f32x4*>(&rec[4 + 8 * c + 4]) = f32x4{acc[4], acc[5], acc[6], acc[7]};
    if (c == 0) { rec[0] = mw; rec[1] = l; }
  }
  __syncthreads();
  if (tid < DH) {
    float M = -INFINITY;
#pragma unroll
    for (int ww = 0; ww < NW; ++ww) M = fmaxf(M, lds[ww * PART_REC]);
    float L = 0.f, o = 0.f;
#pragma unroll
    for (int ww = 0; ww < NW; ++ww) { const float wt = __builtin_amdgcn_exp2f(lds[ww * PART_REC] - M); L = fmaf(wt, lds[ww * PART_REC + 1], L); o = fmaf(wt, lds[ww * PART_REC + 4 + tid], o); }
    float* rec = p.ws + (size_t)((b * H + h) * NSPLIT + s) * PART_REC;
    rec[4 + tid] = o;
    if (tid == 0) { rec[0] = M; rec[1] = L; }
  }
}

__device__ __forceinline__ float bf2f(uint16_t u) { return __builtin_bit_cast(float, (unsigned)u << 16); }
__device__ __forceinline__ uint16_t f2bf(float f) { return (uint16_t)(cvtpk(f, f) & 0xffff); }

constexpr size_t WS_H = 256 * PART_REC;
constexpr size_t WS_ACT = WS_H + B * D;
__global__ void __launch_bounds__(512) wo_kernel(Params p) {
  __shared__ float o[H * DH]; __shared__ float red[8][64];
  const int b = blockIdx.y, cg = blockIdx.x, tid = threadIdx.x;
  for (int i = tid; i < H * DH; i += 512) {
    const int h = i >> 7, d = i & 127;
    const float* r0 = p.ws + (size_t)((b * H + h) * 2) * PART_REC; const float* r1 = r0 + PART_REC;
    const float M = fmaxf(r0[0], r1[0]), w0 = exp2f(r0[0] - M), w1 = exp2f(r1[0] - M);
    const float L = w0 * r0[1] + w1 * r1[1];
    o[i] = rbf((w0 * r0[4 + d] + w1 * r1[4 + d]) / L);
  }
  __syncthreads();
  const int col = cg * 64 + (tid & 63), kq = tid >> 6;
  float a = 0.f;
  for (int k = kq * 512; k < kq * 512 + 512; ++k) a = fmaf(o[k], bf2f(p.wo[(size_t)k * D + col]), a);
  red[kq][tid & 63] = a;
  __syncthreads();
  if (tid < 64) { float t = 0.f; for (int k = 0; k < 8; ++k) t += red[k][tid];
    p.ws[WS_H + b * D + col] = bf2f(p.x[b * D + col]) + t;
  }
}
__device__ __forceinline__ float gelu_tanh(float x) {
  const float u = 0.7978845608028654f * (x + 0.044715f * x * x * x);
  return 0.5f * x * (1.f + tanhf(u));
}
__global__ void __launch_bounds__(256) gu_kernel(Params p) {
  __shared__ float xn[D]; __shared__ float red[8]; __shared__ float rg[8][32], ru[8][32];
  const int b = blockIdx.y, fg = blockIdx.x, tid = threadIdx.x;
  const float* hb = p.ws + WS_H + b * D;
  const float h0 = hb[tid], h1 = hb[tid + 256];
  float sum = wave_sum(h0 + h1); if ((tid & 63) == 0) red[tid >> 6] = sum; __syncthreads();
  const float mean = (red[0] + red[1] + red[2] + red[3]) * (1.f / D); __syncthreads();
  float sq = wave_sum((h0 - mean) * (h0 - mean) + (h1 - mean) * (h1 - mean)); if ((tid & 63) == 0) red[tid >> 6] = sq; __syncthreads();
  const float var = (red[0] + red[1] + red[2] + red[3]) * (1.f / D);
  const float den = sqrtf(var + 1e-5f);
  xn[tid] = rbf((h0 - mean) / den); xn[tid + 256] = rbf((h1 - mean) / den);
  __syncthreads();
  const int j = fg * 32 + (tid & 31), kq = tid >> 5;
  float g = 0.f, u = 0.f;
  for (int k = kq * 64; k < kq * 64 + 64; ++k) { g = fmaf(xn[k], bf2f(p.wg[(size_t)k * F + j]), g); u = fmaf(xn[k], bf2f(p.wu[(size_t)k * F + j]), u); }
  rg[kq][tid & 31] = g; ru[kq][tid & 31] = u;
  __syncthreads();
  if (tid < 32) { float gg = 0.f, uu = 0.f; for (int k = 0; k < 8; ++k) { gg += rg[k][tid]; uu += ru[k][tid]; }
    p.ws[WS_ACT + b * F + j] = rbf(gelu_tanh(gg) * uu);
  }
}
__global__ void __launch_bounds__(512) down_kernel(Params p) {
  __shared__ float red[8][64];
  const int b = blockIdx.y, cg = blockIdx.x, tid = threadIdx.x;
  const int col = cg * 64 + (tid & 63), kq = tid >> 6;
  const float* act = p.ws + WS_ACT + b * F;
  float a = 0.f;
  for (int k = kq * 172; k < kq * 172 + 172; ++k) a = fmaf(act[k], bf2f(p.wd[(size_t)k * D + col]), a);
  red[kq][tid & 63] = a;
  __syncthreads();
  if (tid < 64) { float t = 0.f; for (int k = 0; k < 8; ++k) t += red[k][tid];
    p.out[b * D + col] = f2bf(p.ws[WS_H + b * D + col] + t);
  }
}

extern "C" void kernel_launch(void* const* d_in, const int* in_sizes, int n_in, void* d_out, int out_size, void* d_ws, size_t ws_size,
                              hipStream_t stream) {
  Params p;
  p.x = (const uint16_t*)d_in[0]; p.k = (const uint16_t*)d_in[1]; p.v = (const uint16_t*)d_in[2]; p.wq = (const uint16_t*)d_in[3];
  p.wo = (const uint16_t*)d_in[4]; p.wg = (const uint16_t*)d_in[6]; p.wu = (const uint16_t*)d_in[7]; p.wd = (const uint16_t*)d_in[8];
  p.out = (uint16_t*)d_out; p.ws = (float*)d_ws;
  attn_kernel<<<256, NTHR, 0, stream>>>(p);
  wo_kernel<<<dim3(8, B), 512, 0, stream>>>(p);
  gu_kernel<<<dim3(43, B), 256, 0, stream>>>(p);
  down_kernel<<<dim3(8, B), 512, 0, stream>>>(p);
}
```

```cpp
#include <hip/hip_runtime.h>
#include <hip/hip_bf16.h>
#include <stdint.h>

#define DIAG 0
#define SLP 12
#define KVAUX 2
#define LPER 7000
#define LDEL 100
#define LPEER 16
#define LGMAX 300
#define LCLAMP 3000
#define LWM 5
#define LSTR 4352
#define CSLP 8
#define LEVERY 1600
#define LEV0 600
#define LRD 250
#define CSTOP 3
#define ROT 1
#define QROT 1
#define EAUX 0
#define QAUX 0
#define RHN 8
#define WRD 31
#define PRIO 1
#define HSLP 0
#define Y1SLP 0

constexpr int B = 4, D = 512, H = 32, SK = 8192, DH = 128, F = 1376;
constexpr int NW = 8, NTHR = NW * 64 + 64, QW = 4;
constexpr int KEYS_WG = SK / 2;
constexpr int TILE = 4096;
constexpr int NV = KEYS_WG / 16 / NW;
constexpr int NK = NV;
constexpr int NF = F / 32;

typedef unsigned u32x4 __attribute__((ext_vector_type(4)));
typedef unsigned u32x2 __attribute__((ext_vector_type(2)));
typedef float f32x4 __attribute__((ext_vector_type(4)));
typedef float f32x2 __attribute__((ext_vector_type(2)));
typedef __bf16 bf16x2 __attribute__((ext_vector_type(2)));
typedef __bf16 bf16x8 __attribute__((ext_vector_type(8)));
typedef short i16x4 __attribute__((ext_vector_type(4)));
typedef __amdgpu_buffer_rsrc_t rsrc_t;
#define LDS __attribute__((address_space(3)))

__device__ __forceinline__ float dot2(unsigned a, unsigned b, float acc) {
  return __builtin_amdgcn_fdot2_f32_bf16(__builtin_bit_cast(bf16x2, a), __builtin_bit_cast(bf16x2, b), acc, false);
}
__device__ __forceinline__ float asf(unsigned u) { return __builtin_bit_cast(float, u); }
__device__ __forceinline__ unsigned asu(float f) { return __builtin_bit_cast(unsigned, f); }
__device__ __forceinline__ float bflo(unsigned u) { return asf(u << 16); }
__device__ __forceinline__ float bfhi(unsigned u) { return asf(u & 0xffff0000u); }
__device__ __forceinline__ unsigned cvtpk(float lo, float hi) {
  return __builtin_bit_cast(unsigned, __builtin_convertvector(f32x2{lo, hi}, bf16x2));
}
__device__ __forceinline__ float rbf(float x) { return bflo(cvtpk(x, x)); }
template <int CTRL> __device__ __forceinline__ float dpp(float x) {
  return asf(__builtin_amdgcn_mov_dpp((int)asu(x), CTRL, 0xf, 0xf, true));
}
template <int CTRL> __device__ __forceinline__ unsigned dppu(unsigned x) { return (unsigned)__builtin_amdgcn_mov_dpp((int)x, CTRL, 0xf, 0xf, true); }
constexpr int XOR1 = 0xB1, XOR2 = 0x4E, XOR7 = 0x141, XOR8 = 0x128, ROR4 = 0x124, BCAST0 = 0x150;
__device__ __forceinline__ float tsum32(float a, float b) { auto s = __builtin_amdgcn_permlane32_swap(asu(a), asu(b), false, false); return asf(s[0]) + asf(s[1]); }
__device__ __forceinline__ float tsum16(float a, float b) { auto s = __builtin_amdgcn_permlane16_swap(asu(a), asu(b), false, false); return asf(s[0]) + asf(s[1]); }
__device__ __forceinline__ float xrow_max(float x) {
  auto s = __builtin_amdgcn_permlane16_swap(asu(x), asu(x), false, false);
  x = fmaxf(asf(s[0]), asf(s[1]));
  auto t = __builtin_amdgcn_permlane32_swap(asu(x), asu(x), false, false);
  return fmaxf(asf(t[0]), asf(t[1]));
}
__device__ __forceinline__ float xrow_sum(float x) { x = tsum16(x, x); return tsum32(x, x); }
__device__ __forceinline__ float row_sum16(float x) { x += dpp<XOR8>(x); x += dpp<XOR7>(x); x += dpp<XOR2>(x); x += dpp<XOR1>(x); return x; }
__device__ __forceinline__ float wave_sum(float x) { return xrow_sum(row_sum16(x)); }
__device__ __forceinline__ f32x4 mfma(const u32x4& a, const u32x4& b, const f32x4& c) {
  return __builtin_amdgcn_mfma_f32_16x16x32_bf16(__builtin_bit_cast(bf16x8, a), __builtin_bit_cast(bf16x8, b), c, 0, 0, 0);
}
__device__ __forceinline__ u32x4 tr_operand(LDS const unsigned char* p0, LDS const unsigned char* p1) {
  const u32x2 a = __builtin_bit_cast(u32x2, __builtin_amdgcn_ds_read_tr16_b64_v4i16((LDS i16x4*)p0));
  const u32x2 b = __builtin_bit_cast(u32x2, __builtin_amdgcn_ds_read_tr16_b64_v4i16((LDS i16x4*)p1));
  return u32x4{a[0], a[1], b[0], b[1]};
}
__device__ __forceinline__ float row_pick(float a0, float a1, float a2, float a3, int lane) {
  const float x = (lane & 16) ? a1 : a0, y = (lane & 16) ? a3 : a2;
  return (lane & 32) ? y : x;
}

__device__ __forceinline__ rsrc_t mkrsrc(const void* p, unsigned bytes) { return __builtin_amdgcn_make_buffer_rsrc((void*)p, 0, bytes, 0x00020000); }
constexpr int AUX_NT = 2, AUX_SC1 = 16;
template <int AUX> __device__ __forceinline__ u32x4 bload(rsrc_t r, int voff, int soff) { return __builtin_amdgcn_raw_buffer_load_b128(r, voff, soff, AUX); }
template <int AUX> __device__ __forceinline__ u32x2 bload8(rsrc_t r, int voff, int soff) { return __builtin_amdgcn_raw_buffer_load_b64(r, voff, soff, AUX); }
__device__ __forceinline__ void sb() { __builtin_amdgcn_sched_barrier(0); }
__device__ __forceinline__ void wg_barrier() { asm volatile("s_waitcnt lgkmcnt(0)\n\ts_barrier" ::: "memory"); }

constexpr int RH = RHN;
constexpr int WS_EPOCH = 0;
constexpr int WS_CLR = 1024;
constexpr int WS_ML = 4096;
constexpr int WS_HH = 8192;
constexpr int WS_Y1 = WS_HH + B * RH * 1024;
constexpr int WS_Y2 = WS_Y1 + B * 64 * 2048;
constexpr int WS_LAG = (WS_Y2 + B * 64 * 2048 + 4095) & ~4095;
constexpr int WS_END = WS_LAG + 256 * (LSTR > 8 ? LSTR : 8);
constexpr unsigned EMPTY = 0xFFFFFFFFu;
constexpr unsigned SPIN_MAX = 1u << 18;
__device__ __forceinline__ unsigned canon(unsigned u) { return u < 0xFFFFFFFEu ? u : 0xFFFFFFFEu; }
__device__ __forceinline__ bool full4(const u32x4& v) { return (v[0] != EMPTY) & (v[1] != EMPTY) & (v[2] != EMPTY) & (v[3] != EMPTY); }

constexpr int RBS = 320;
constexpr int L_RB = 0;
constexpr int L_ML = 2560;
constexpr int L_ST = 2624;
constexpr int L_LAG = 2688;
constexpr int L_GU = 2816;
constexpr int L_CS = L_GU + 2048;
constexpr int L_S = 8192;
constexpr int L_E = L_S + KEYS_WG * 4;
constexpr int L_END = L_E + NW * 16384;

template <bool V> struct BoolC { static constexpr bool value = V; };
struct Params {
  const uint16_t* x; const uint16_t* k; const uint16_t* v; const uint16_t* wq; const uint16_t* wo;
  const uint16_t* wg; const uint16_t* wu; const uint16_t* wd; uint16_t* out; unsigned char* ws;
};

#define STAMP(i) do {} while (0)
#define PROG(t) do {} while (0)

__device__ __forceinline__ float dots(const u32x4& K, const u32x4& q) {
  float d = dot2(K[0], q[0], 0.f);
  d = dot2(K[1], q[1], d); d = dot2(K[2], q[2], d); return dot2(K[3], q[3], d);
}
__device__ __forceinline__ float score(float x0, float x1, float x2, float x3, int c) {
  const float a0 = x0 + dpp<XOR8>(x0), a2 = x2 + dpp<XOR8>(x2), a1 = x1 + dpp<XOR8>(x1), a3 = x3 + dpp<XOR8>(x3);
  const float y0 = (c & 8) ? a2 : a0, y1 = (c & 8) ? a3 : a1;
  const float t1 = y0 + dpp<XOR7>(y0), t2 = y1 + dpp<XOR7>(y1);
  float s = (c & 4) ? t2 : t1;
  s += dpp<XOR2>(s); s += dpp<XOR1>(s);
  return rbf(rbf(s) * 0.088388347648318440f) * 1.4426950408889634f;
}
__device__ __forceinline__ void pv(const u32x4& V, float pr, f32x2 (&acc)[4]) {
#pragma unroll
  for (int k = 0; k < 4; ++k) acc[k] = __builtin_elementwise_fma(f32x2{pr, pr}, f32x2{bflo(V[k]), bfhi(V[k])}, acc[k]);
}
__device__ __forceinline__ void pin4(f32x2 (&a)[4]) { asm volatile("" : "+v"(a[0]), "+v"(a[1]), "+v"(a[2]), "+v"(a[3]) :: "memory"); }
template <int N> __device__ __forceinline__ void issue(u32x4 (&T)[N], rsrc_t r, int voff, int soff) {
#pragma unroll
  for (int i = 0; i < N; ++i) T[i] = bload<KVAUX>(r, voff + i * 1024, soff);
  sb();
}
__device__ __forceinline__ void pair_rows(const u32x4& A, const u32x4& Bv, unsigned (&o)[8]) {
#pragma unroll
  for (int k = 0; k < 4; ++k) { o[2 * k] = __builtin_amdgcn_perm(Bv[k], A[k], 0x05040100u); o[2 * k + 1] = __builtin_amdgcn_perm(Bv[k], A[k], 0x07060302u); }
}
__device__ __forceinline__ float reduce_par(float t0, float t1, float t2, float t3) {
  float v = tsum16(tsum32(t0, t2), tsum32(t1, t3));
  v += dpp<XOR8>(v); v += dpp<ROR4>(v); v += dpp<XOR2>(v);
  return v;
}
__device__ __forceinline__ unsigned pack_rows(float x) { auto s = __builtin_amdgcn_permlane16_swap(asu(x), asu(x), false, false); return cvtpk(asf(s[0]), asf(s[1])); }
__device__ __forceinline__ void ln_stats(const float (&xs)[8], float& mean, float& rden) {
  float sum = 0.f;
#pragma unroll
  for (int k = 0; k < 8; ++k) sum += xs[k];
  const float mu = wave_sum(sum) * (1.f / D);
  float sq = 0.f;
#pragma unroll
  for (int k = 0; k < 8; ++k) { const float d = xs[k] - mu; sq = fmaf(d, d, sq); }
  const float var = wave_sum(sq) * (1.f / D);
  mean = rbf(mu);
  rden = rbf(sqrtf(rbf(rbf(var) + 1e-5f)));
}
__device__ __forceinline__ float max8(float m) { m = fmaxf(m, dpp<XOR7>(m)); m = fmaxf(m, dpp<XOR2>(m)); return fmaxf(m, dpp<XOR1>(m)); }

__global__ void __launch_bounds__(NTHR, 2) fused_kernel(Params p) {
  __shared__ __attribute__((aligned(1024))) unsigned char smem[L_END];
  LDS unsigned char* const sm = (LDS unsigned char*)smem;
  LDS float* const l_q = (LDS float*)(sm + L_RB);
  LDS u32x2* const l_ml = (LDS u32x2*)(sm + L_ML);
  LDS unsigned* const l_lag = (LDS unsigned*)(sm + L_LAG);
  LDS float* const l_gu = (LDS float*)(sm + L_GU);
  LDS float* const l_cs = (LDS float*)(sm + L_CS);
  LDS float* const l_s = (LDS float*)(sm + L_S);
  const int tid = threadIdx.x, lane = tid & 63, w = __builtin_amdgcn_readfirstlane(tid >> 6), r = lane >> 4, c = lane & 15;
  const int blk = blockIdx.x;
  const int h = 4 * (blk & 7) + (blk >> 6), b = (blk >> 3) & 3, s = (blk >> 5) & 1;
  const int idx = 2 * h + s;
  const unsigned t_start = (unsigned)__builtin_amdgcn_s_memrealtime();
  STAMP(0);
  asm volatile("" :: "s"(p.x), "s"(p.k), "s"(p.v), "s"(p.wq), "s"(p.wo), "s"(p.wg), "s"(p.wu), "s"(p.wd), "s"(p.out), "s"(p.ws));
  constexpr unsigned LP = LPER / (NK + NV);
  unsigned sched = LP;
  const size_t kvoff = ((size_t)(b * H + h) * SK + (size_t)s * KEYS_WG) * DH;
  const rsrc_t rk = mkrsrc(p.k + kvoff, KEYS_WG * DH * 2), rv = mkrsrc(p.v + kvoff, KEYS_WG * DH * 2);
  const rsrc_t rq = mkrsrc(p.wq + h * DH, D * H * DH * 2 - h * DH * 2);
  const rsrc_t rws = mkrsrc(p.ws, WS_END);
  const int j = b * 64 + idx;
  const bool ffn = idx < NF;
  const int vhh = WS_HH + (b * RH + (idx & (RH - 1))) * 1024;
  if (w == NW) {
    if (lane < 32) l_lag[lane] = lane == 9 ? 0u : t_start - 100000u;
    if (lane < NW) l_ml[lane] = u32x2{EMPTY, EMPTY};
    const u32x4 e4 = {EMPTY, EMPTY, EMPTY, EMPTY};
#pragma unroll
    for (int i = 0; i < 2; ++i) __builtin_amdgcn_raw_buffer_store_b128(e4, rws, WS_Y1 + j * 2048 + i * 1024 + lane * 16, 0, AUX_SC1);
#pragma unroll
    for (int i = 0; i < 2; ++i) __builtin_amdgcn_raw_buffer_store_b128(e4, rws, WS_Y2 + j * 2048 + i * 1024 + lane * 16, 0, AUX_SC1);
    __builtin_amdgcn_raw_buffer_store_b128(e4, rws, lane < RH ? WS_HH + (b * RH + lane) * 1024 + idx * 16 : lane == RH ? WS_ML + j * 16 : WS_END, 0, AUX_SC1);
    sb();
    u32x2 er = bload8<AUX_SC1>(rws, WS_EPOCH + b * 256, 0);
    unsigned xres = p.x[b * D + 8 * idx + 4 * (c & 1) + r];
    sb();
    wg_barrier();
    asm volatile("s_waitcnt vmcnt(0)" : "+v"(er), "+v"(xres) :: "memory");
    const unsigned long long tg = ((unsigned long long)er[0] | ((unsigned long long)er[1] << 32)) + 1ull;
    const u32x2 tg2 = {(unsigned)tg, (unsigned)(tg >> 32)};
    __builtin_amdgcn_raw_buffer_store_b64(tg2, rws, lane == 0 ? WS_CLR + j * 8 : WS_END, 0, AUX_SC1);
    bool cok = false, mlp = false;
    auto publish_ml = [&]() {
      const u32x2 ml = l_ml[lane & 7];
      if (__ballot((ml[0] == EMPTY) | (ml[1] == EMPTY)) == 0ull) {
        const float M = max8(asf(ml[0]));
        float L = __builtin_amdgcn_exp2f(asf(ml[0]) - M) * asf(ml[1]);
        L += dpp<XOR7>(L); L += dpp<XOR2>(L); L += dpp<XOR1>(L);
        __builtin_amdgcn_raw_buffer_store_b64(u32x2{canon(asu(M)), canon(asu(L))}, rws, lane == 0 ? WS_ML + j * 16 : WS_END, 0, AUX_SC1);
        mlp = true;
      }
    };
    {
      const int lag_st = lane == 0 ? WS_LAG + blk * LSTR : WS_END;
      const int lag_ld = lane < LPEER ? WS_LAG + ((blk / LPEER) * LPEER + lane) * LSTR : WS_END;
      unsigned next = t_start + LEV0 + ((blk / LPEER) * LEVERY) / (256 / LPEER), gref = t_start - 100000u;
      unsigned nextr = next + LRD;
#pragma unroll 1
      for (int it = 0; it < 4096; ++it) {
        const unsigned stop = l_lag[9];
        const unsigned mine = l_lag[lane & 7], ref0 = __builtin_amdgcn_readfirstlane(mine);
        int d = (int)(mine - ref0);
        d += (int)dppu<XOR7>((unsigned)d); d += (int)dppu<XOR2>((unsigned)d); d += (int)dppu<XOR1>((unsigned)d); d >>= 3;
        const unsigned own = ref0 + (unsigned)d;
        const unsigned tnow = (unsigned)__builtin_amdgcn_s_memrealtime();
        if ((int)(tnow - next) >= 0) {
          next += LEVERY;
          __builtin_amdgcn_raw_buffer_store_b64(u32x2{own, ~own}, rws, lag_st, 0, AUX_SC1);
        }
        if ((int)(tnow - nextr) >= 0) {
          nextr += LEVERY;
          const u32x2 cm = bload8<AUX_SC1>(rws, lag_ld, 0);
          if (!cok) {
            const u32x2 cf = bload8<AUX_SC1>(rws, WS_CLR + (b * 64 + lane) * 8, 0);
            cok = __ballot((cf[0] != tg2[0]) | (cf[1] != tg2[1])) == 0ull;
          }
          int e = (int)(cm[0] - own);
          e = (cm[0] == ~cm[1] && e < LCLAMP) ? e : 0;
          e = max(e, (int)dppu<XOR8>((unsigned)e)); e = max(e, (int)dppu<XOR7>((unsigned)e)); e = max(e, (int)dppu<XOR2>((unsigned)e)); e = max(e, (int)dppu<XOR1>((unsigned)e));
          if constexpr (LPEER >= 32) { auto sx = __builtin_amdgcn_permlane16_swap((unsigned)e, (unsigned)e, false, false); e = max((int)sx[0], (int)sx[1]); }
          if constexpr (LPEER >= 64) { auto sx = __builtin_amdgcn_permlane32_swap((unsigned)e, (unsigned)e, false, false); e = max((int)sx[0], (int)sx[1]); }
          gref = own + (unsigned)e;
        }
        const int e2 = max((int)(__builtin_amdgcn_readfirstlane(gref) - own), 0);
        if (lane == 0) l_lag[8] = own + (unsigned)e2;
        if (!mlp) publish_ml();
        if (__builtin_amdgcn_readfirstlane(stop) != 0u) break;
        __builtin_amdgcn_s_sleep(CSLP);
        asm volatile("" ::: "memory");
      }
    }
    for (unsigned sp = 0; sp < SPIN_MAX && !mlp; ++sp) { publish_ml(); asm volatile("" ::: "memory"); }
    for (unsigned sp = 0; sp < SPIN_MAX && !cok; ++sp) {
      const u32x2 cf = bload8<AUX_SC1>(rws, WS_CLR + (b * 64 + lane) * 8, 0);
      cok = __ballot((cf[0] != tg2[0]) | (cf[1] != tg2[1])) == 0ull;
      if (!cok) __builtin_amdgcn_s_sleep(8);
      asm volatile("" ::: "memory");
    }
    __builtin_amdgcn_raw_buffer_store_b64(tg2, rws, (idx == 0 && lane == 0) ? WS_EPOCH + b * 256 : WS_END, 0, AUX_SC1);
    float cf0, cf1;
    {
      u32x2 m0, m1;
      for (unsigned sp = 0; sp < SPIN_MAX; ++sp) {
        m0 = bload8<AUX_SC1>(rws, WS_ML + (b * 64 + (lane >> 1)) * 16, 0);
        m1 = bload8<AUX_SC1>(rws, WS_ML + (b * 64 + 32 + (lane >> 1)) * 16, 0);
        asm volatile("" ::: "memory");
        if (__ballot((m0[0] == EMPTY) | (m0[1] == EMPTY) | (m1[0] == EMPTY) | (m1[1] == EMPTY)) == 0ull) break;
        __builtin_amdgcn_s_sleep(8);
      }
      const float a0 = asf(m0[0]), e0 = __builtin_amdgcn_exp2f(a0 - fmaxf(a0, dpp<XOR2>(a0))), l0 = asf(m0[1]) * e0;
      const float a1 = asf(m1[0]), e1 = __builtin_amdgcn_exp2f(a1 - fmaxf(a1, dpp<XOR2>(a1))), l1 = asf(m1[1]) * e1;
      cf0 = e0 / (l0 + dpp<XOR2>(l0)); cf1 = e1 / (l1 + dpp<XOR2>(l1));
      asm volatile("" : "+v"(cf0), "+v"(cf1) :: "memory");
    }
    if (PRIO) __builtin_amdgcn_s_setprio(3);
    wg_barrier();
    STAMP(7);
    if (Y1SLP) __builtin_amdgcn_s_sleep(Y1SLP);
    float hres;
    {
      const int vy = WS_Y1 + j * 2048 + lane * 16;
      u32x4 ya = {EMPTY, EMPTY, EMPTY, EMPTY}, yb = ya;
      bool ok = false;
      for (unsigned sp = 0; sp < SPIN_MAX; ++sp) {
        if (!ok) { ya = bload<AUX_SC1>(rws, vy, 0); yb = bload<AUX_SC1>(rws, vy + 1024, 0); ok = full4(ya) & full4(yb); }
        asm volatile("" ::: "memory");
        const int miss = __popcll(__ballot(!ok));
        if (miss == 0) break;
        if (miss > 16) __builtin_amdgcn_s_sleep(SLP);
      }
      STAMP(9);
      const float sum = reduce_par(fmaf(cf1, asf(yb[0]), cf0 * asf(ya[0])), fmaf(cf1, asf(yb[1]), cf0 * asf(ya[1])),
                                   fmaf(cf1, asf(yb[2]), cf0 * asf(ya[2])), fmaf(cf1, asf(yb[3]), cf0 * asf(ya[3])));
      const unsigned pk = pack_rows(bflo(xres) + rbf(sum));
      const int rep = (c >> 1) + 8 * (r & 1);
      __builtin_amdgcn_raw_buffer_store_b32(canon(pk), rws, rep < RH ? WS_HH + (b * RH + rep) * 1024 + idx * 16 + (2 * (c & 1) + (r >> 1)) * 4 : WS_END, 0, AUX_SC1);
      hres = (r & 1) ? bfhi(pk) : bflo(pk);
      STAMP(10);
    }
    if (ffn) {
      wg_barrier();
      if (HSLP) __builtin_amdgcn_s_sleep(HSLP);
      u32x4 hv;
      for (unsigned sp = 0; sp < SPIN_MAX; ++sp) {
        hv = bload<AUX_SC1>(rws, vhh + lane * 16, 0);
        asm volatile("" ::: "memory");
        if (__ballot(!full4(hv)) == 0ull) break;
      }
      STAMP(11);
      const float sh = bflo(__builtin_amdgcn_readfirstlane(hv[0]));
      float s1 = 0.f, s2 = 0.f;
#pragma unroll
      for (int k = 0; k < 4; ++k) {
        const float d0 = bflo(hv[k]) - sh, d1 = bfhi(hv[k]) - sh;
        s1 += d0 + d1; s2 = fmaf(d0, d0, fmaf(d1, d1, s2));
      }
      const float m1 = wave_sum(s1) * (1.f / D), m2 = wave_sum(s2) * (1.f / D);
      const float var = fmaxf(fmaf(-m1, m1, m2), 0.f);
      const float den = rbf(__builtin_amdgcn_sqrtf(rbf(rbf(var) + 1e-5f)));
      if (lane == 0) *(LDS f32x2*)(sm + L_ST) = f32x2{rbf(sh + m1), __builtin_amdgcn_rcpf(den)};
      STAMP(3);
      wg_barrier();
    }
    {
      const int vy = WS_Y2 + j * 2048 + lane * 16, vz = lane < 2 * (NF - 32) ? vy + 1024 : WS_END;
      u32x4 ya = {0, 0, 0, 0}, yb = ya;
      bool ok = false;
      for (unsigned sp = 0; sp < SPIN_MAX; ++sp) {
        if (!ok) { ya = bload<AUX_SC1>(rws, vy, 0); yb = bload<AUX_SC1>(rws, vz, 0); ok = full4(ya) & full4(yb); }
        asm volatile("" ::: "memory");
        const int miss = __popcll(__ballot(!ok));
        if (miss == 0) break;
        if (miss > 16) __builtin_amdgcn_s_sleep(SLP / 2);
      }
      STAMP(13);
      const float sum = reduce_par(asf(ya[0]) + asf(yb[0]), asf(ya[1]) + asf(yb[1]), asf(ya[2]) + asf(yb[2]), asf(ya[3]) + asf(yb[3]));
      const unsigned pk = pack_rows(hres + rbf(sum));
      if ((lane & 0x1e) == 0) *reinterpret_cast<unsigned*>(p.out + b * D + idx * 8 + 4 * (c & 1) + (r & 2)) = pk;
      STAMP(14);
    }
    return;
  }
  u32x4 X[2][2];
  const int voff = lane * 16;
  constexpr int tstride = NW * TILE;
  constexpr int WRAP = KEYS_WG * DH * 2 - 1;
  const int rot = ROT ? ((h & 7) * ROT + 8 * b + s + 2 * (h >> 3)) & 31 : 0;
  const int rbase = rot * tstride;
  int soff = (w * TILE + rbase) & WRAP;
  auto fill = [&]() { issue(X[0], rk, voff, soff); issue(X[1], rk, voff + 2048, soff); };
  if (w < 4) {
    const u32x4 xv = *reinterpret_cast<const u32x4*>(p.x + b * D + lane * 8);
    const int qr = QROT * 4 * b;
    const unsigned xo = *reinterpret_cast<const unsigned*>(p.x + b * D + 128 * w + 8 * ((c + qr) & 15) + 2 * r);
    sb();
    u32x4 W[32];
    const int vo = 2 * r * 8192 + c * 16, so = w * 128 * 8192;
#pragma unroll
    for (int i = 0; i < 32; ++i) W[i] = bload<QAUX>(rq, vo, so + (8 * (((i >> 1) + qr) & 15) + (i & 1)) * 8192);
    sb();
    float xs[8];
#pragma unroll
    for (int k = 0; k < 4; ++k) { xs[2 * k] = bflo(xv[k]); xs[2 * k + 1] = bfhi(xv[k]); }
    float mean, den; ln_stats(xs, mean, den);
    unsigned xn = cvtpk(rbf(bflo(xo) - mean) / den, rbf(bfhi(xo) - mean) / den);
    asm volatile("" : "+v"(xn) :: "memory");
    sb();
    float a[8];
#pragma unroll
    for (int k = 0; k < 8; ++k) a[k] = 0.f;
#define QP(pp) { unsigned t[8]; pair_rows(W[2 * pp], W[2 * pp + 1], t); const unsigned xb = dppu<BCAST0 + pp>(xn); \
                 _Pragma("unroll") for (int e = 0; e < 8; ++e) a[e] = dot2(t[e], xb, a[e]); }
    QP(0) QP(1) QP(2) QP(3) QP(4) QP(5) QP(6) QP(7)
    QP(8) QP(9) QP(10) QP(11) QP(12) QP(13) QP(14) QP(15)
#undef QP
    float u[4], v[2];
#pragma unroll
    for (int e = 0; e < 4; ++e) u[e] = tsum32(a[e], a[e + 4]);
#pragma unroll
    for (int e = 0; e < 2; ++e) v[e] = tsum16(u[e], u[e + 2]);
    *(LDS f32x2*)&l_q[w * DH + 8 * c + 4 * (lane >> 5) + 2 * (r & 1)] = f32x2{v[0], v[1]};
  } else {
    fill();
  }
  wg_barrier();
  u32x4 q;
  {
    f32x4 a = {0, 0, 0, 0}, bq = {0, 0, 0, 0};
#pragma unroll
    for (int ww = 0; ww < QW; ++ww) {
      a += *(LDS const f32x4*)&l_q[ww * DH + 8 * c];
      bq += *(LDS const f32x4*)&l_q[ww * DH + 8 * c + 4];
    }
    q[0] = cvtpk(a[0], a[1]); q[1] = cvtpk(a[2], a[3]); q[2] = cvtpk(bq[0], bq[1]); q[3] = cvtpk(bq[2], bq[3]);
    asm volatile("" : "+v"(q[0]), "+v"(q[1]), "+v"(q[2]), "+v"(q[3]) :: "memory");
  }
  STAMP(4);
  if (w < 4) fill();
  auto gate = [&](unsigned lref) {
    const unsigned now = (unsigned)__builtin_amdgcn_s_memrealtime(), lag = now - sched;
    if (lane == 0) l_lag[w] = lag;
    const int wt = (int)(__builtin_amdgcn_readfirstlane(lref) - LDEL - lag);
    if (wt > 0) {
#pragma unroll 1
      for (int k = (min(wt, LGMAX) * LWM) >> 4; k > 0; --k) __builtin_amdgcn_s_sleep(1);
    }
    sched += LP;
    sb();
  };
  const int sl = w * 512 + r * 4 + (c >> 2);
  const unsigned gsz = ffn ? D * F * 2 - idx * 64 : 0, dsz = ffn ? 32 * D * 2 : 0;
  const int vgu = (8 * (lane >> 4) + ((lane >> 2) & 3)) * F * 2 + (lane & 3) * 16, sgu = w * 64 * F * 2;
  const int vd = (8 * (lane >> 5) + ((lane >> 3) & 3)) * 1024 + (lane & 7) * 16, sd = w * 128;
  LDS unsigned char* const eb = sm + L_E + w * 16384;
  LDS unsigned char* const park_gu = eb + lane * 16;
  LDS unsigned char* const park_d = eb + (lane >> 3) * 128 + (((lane & 7) ^ ((lane >> 3) & 6)) * 16);
  constexpr int NFE = 12, NE = NFE + 16;
  u32x4 E;
  auto eload = [&](int e) {
    const int i = e - NFE;
    const bool gu = e < 8;
    const uint16_t* base = i >= 0 ? p.wo + (size_t)h * DH * D : e < 4 ? p.wg + idx * 32 : e < 8 ? p.wu + idx * 32 : p.wd + (size_t)(ffn ? idx : 0) * 32 * D;
    const unsigned size = e >= NE ? 0u : i >= 0 ? DH * D * 2 : gu ? gsz : dsz;
    const int k = i >= 0 ? i : e & 3;
    const int so = gu ? sgu + (32 * (k >> 1) + 4 * (k & 1)) * F * 2 : sd + (32 * (k >> 2) + 16 * (k & 1) + 4 * ((k >> 1) & 1)) * 1024;
    E = bload<EAUX>(mkrsrc(base, size), gu ? vgu : vd, so);
    sb();
  };
  auto kround = [&](int tt, rsrc_t rn, int son, bool first_e = false) {
    const unsigned lref = l_lag[8];
    float x0 = dots(X[0][0], q), x1 = dots(X[0][1], q);
    asm volatile("" : "+v"(x0), "+v"(x1) :: "memory");
    sb();
    gate(lref);
    issue(X[0], rn, voff, son);
    if (first_e) eload(0);
    float x2 = dots(X[1][0], q), x3 = dots(X[1][1], q);
    asm volatile("" : "+v"(x2), "+v"(x3) :: "memory");
    sb();
    issue(X[1], rn, voff + 2048, son);
    l_s[sl + tt * 16] = score(x0, x1, x2, x3, c);
  };
  soff = (soff + tstride) & WRAP;
  kround(0, rk, soff);
  PROG(0);
#pragma unroll 1
  for (int t = 1; t < NK - 1; ++t) { soff = (soff + tstride) & WRAP; kround(t, rk, soff); PROG(t); }
  soff = w * TILE + rbase;
  kround(NV - 1, rv, soff, true);
  PROG(NV - 1);
  float mw;
  {
    asm volatile("" ::: "memory");
    f32x4 a = *(LDS const f32x4*)&l_s[w * 512 + lane * 8], bb = *(LDS const f32x4*)&l_s[w * 512 + lane * 8 + 4];
    float m = fmaxf(fmaxf(fmaxf(a[0], a[1]), fmaxf(a[2], a[3])), fmaxf(fmaxf(bb[0], bb[1]), fmaxf(bb[2], bb[3])));
    m = fmaxf(m, dpp<XOR8>(m)); m = fmaxf(m, dpp<XOR7>(m)); m = fmaxf(m, dpp<XOR2>(m)); m = fmaxf(m, dpp<XOR1>(m));
    mw = xrow_max(m);
#pragma unroll
    for (int e = 0; e < 4; ++e) { a[e] = __builtin_amdgcn_exp2f(a[e] - mw); bb[e] = __builtin_amdgcn_exp2f(bb[e] - mw); }
    *(LDS f32x4*)&l_s[w * 512 + lane * 8] = a; *(LDS f32x4*)&l_s[w * 512 + lane * 8 + 4] = bb;
    const float lw = wave_sum(((a[0] + a[1]) + (a[2] + a[3])) + ((bb[0] + bb[1]) + (bb[2] + bb[3])));
    if (lane == 0) l_ml[w] = u32x2{canon(asu(mw)), canon(asu(lw))};
    asm volatile("" : "+v"(mw) :: "memory");
  }
  f32x2 acc[4];
#pragma unroll
  for (int k = 0; k < 4; ++k) acc[k] = f32x2{0.f, 0.f};
  const int pl = w * 512 + r * 4;
  constexpr int NR = NV - 1;
  auto vround = [&](int t, auto parks) {
    constexpr bool PARK = decltype(parks)::value;
    soff = (soff + tstride) & WRAP;
    const unsigned lref = l_lag[8];
    const f32x4 p4 = *(LDS const f32x4*)&l_s[pl + t * 16];
    pv(X[0][0], p4[0], acc); pv(X[0][1], p4[1], acc);
    pin4(acc);
    if constexpr (PARK) *(LDS u32x4*)((t < 8 ? park_gu : park_d) + (t < NFE ? t : t - NFE) * 1024) = E;
    asm volatile("" ::: "memory");
    sb();
    gate(lref);
    issue(X[0], rv, voff, soff);
    if constexpr (PARK) eload(t + 1);
    pv(X[1][0], p4[2], acc); pv(X[1][1], p4[3], acc);
    pin4(acc);
    sb();
    issue(X[1], rv, voff + 2048, soff);
    if (t == NR - CSTOP && lane == 0) l_lag[9] = 1u;
    PROG(NV + t);
  };
#pragma unroll 1
  for (int t = 0; t < NFE; ++t) vround(t, BoolC<true>{});
  const int kq = r, tq = (lane >> 2) & 3, tp = lane & 3;
  const int g8 = 4 * (kq & 1) + tq;
  LDS const unsigned char* const tr_d = eb + (kq >> 1) * 1024 + g8 * 128 + (((tp >> 1) ^ (g8 & 6)) * 16) + (tp & 1) * 8;
  auto trd = [&](int t) { return (LDS const unsigned char*)(size_t)((unsigned)(size_t)tr_d ^ (unsigned)(t * 32)); };
  u32x4 GU[2][4], Dn[4];
  {
    LDS const unsigned char* const tr_g = eb + (tp >> 1) * 4096 + (4 * kq + tq) * 64 + (tp & 1) * 8;
#pragma unroll
    for (int ks = 0; ks < 2; ++ks)
#pragma unroll
      for (int t = 0; t < 4; ++t) GU[ks][t] = tr_operand(tr_g + 2 * ks * 1024 + t * 16, tr_g + (2 * ks + 1) * 1024 + t * 16);
#pragma unroll
    for (int t = 0; t < 4; ++t) Dn[t] = tr_operand(trd(t) + 8 * 1024, trd(t) + 10 * 1024);
    const u32x4 ones = {0x3F803F80u, 0x3F803F80u, 0x3F803F80u, 0x3F803F80u};
    f32x4 cs[4];
#pragma unroll
    for (int t = 0; t < 4; ++t) cs[t] = mfma(ones, GU[1][t], mfma(ones, GU[0][t], f32x4{0.f, 0.f, 0.f, 0.f}));
    l_cs[w * 64 + lane] = row_pick(cs[0][0], cs[1][0], cs[2][0], cs[3][0], lane);
#pragma unroll
    for (int t = 0; t < 4; ++t) asm volatile("" : "+v"(GU[0][t]), "+v"(GU[1][t]), "+v"(Dn[t]) :: "memory");
  }
#pragma unroll 1
  for (int t = NFE; t < NE; ++t) vround(t, BoolC<true>{});
  float sc;
  {
    u32x2 ml;
    for (unsigned sp = 0; sp < SPIN_MAX; ++sp) {
      ml = l_ml[lane & 7];
      asm volatile("" ::: "memory");
      if (__ballot((ml[0] == EMPTY) | (ml[1] == EMPTY)) == 0ull) break;
    }
    sc = __builtin_amdgcn_exp2f(mw - max8(asf(ml[0])));
    asm volatile("" : "+v"(sc) :: "memory");
  }
  u32x4 Wo[4][4];
  auto read_wo = [&]() {
#pragma unroll
    for (int ks = 0; ks < 4; ++ks)
#pragma unroll
      for (int t = 0; t < 4; ++t) Wo[ks][t] = tr_operand(trd(t) + 4 * ks * 1024, trd(t) + (4 * ks + 2) * 1024);
#pragma unroll
    for (int ks = 0; ks < 4; ++ks) asm volatile("" : "+v"(Wo[ks][0]), "+v"(Wo[ks][1]), "+v"(Wo[ks][2]), "+v"(Wo[ks][3]) :: "memory");
  };
#pragma unroll 1
  for (int t = NE; t < WRD; ++t) vround(t, BoolC<false>{});
  read_wo();
#pragma unroll 1
  for (int t = WRD; t < NR; ++t) vround(t, BoolC<false>{});
  {
    const f32x4 p4 = *(LDS const f32x4*)&l_s[pl + NR * 16];
    pv(X[0][0], p4[0], acc); pv(X[0][1], p4[1], acc);
    pin4(acc);
    sb();
    pv(X[1][0], p4[2], acc); pv(X[1][1], p4[3], acc);
    pin4(acc);
    sb();
  }
  PROG(NV + NR);
  STAMP(5);
  {
    float u[4], v[2];
#pragma unroll
    for (int e = 0; e < 4; ++e) u[e] = tsum32(acc[e >> 1][e & 1], acc[2 + (e >> 1)][e & 1]);
#pragma unroll
    for (int e = 0; e < 2; ++e) v[e] = tsum16(u[e], u[e + 2]);
    *(LDS unsigned*)(sm + L_RB + w * RBS + (4 * c + 2 * (lane >> 5) + (r & 1)) * 4) = cvtpk(v[0] * sc, v[1] * sc);
  }
  STAMP(6);
  wg_barrier();
  STAMP(7);
  const int vyo = (b * 64 + 8 * w + (lane >> 3)) * 2048 + idx * 32 + (lane & 7) * 4;
  {
    u32x4 A[4];
#pragma unroll
    for (int ks = 0; ks < 4; ++ks) A[ks] = *(LDS const u32x4*)(sm + L_RB + (lane & 7) * RBS + kq * 16 + ks * 64);
    f32x4 C[4];
#pragma unroll
    for (int t = 0; t < 4; ++t) C[t] = mfma(A[0], Wo[0][t], f32x4{0.f, 0.f, 0.f, 0.f});
#pragma unroll
    for (int ks = 1; ks < 4; ++ks)
#pragma unroll
      for (int t = 0; t < 4; ++t) C[t] = mfma(A[ks], Wo[ks][t], C[t]);
    float sv[4];
#pragma unroll
    for (int t = 0; t < 4; ++t) sv[t] = (C[t][0] + C[t][1]) + (C[t][2] + C[t][3]);
    const float v01 = tsum16(sv[0], sv[1]), v23 = tsum16(sv[2], sv[3]);
    __builtin_amdgcn_raw_buffer_store_b32(canon(asu((lane & 32) ? v23 : v01)), rws, WS_Y1 + vyo, 0, AUX_SC1);
  }
  STAMP(8);
  if (ffn) {
    float csum = 0.f;
#pragma unroll
    for (int ww = 0; ww < NW; ++ww) csum += l_cs[ww * 64 + lane];
    asm volatile("" : "+v"(csum) :: "memory");
    wg_barrier();
    STAMP(2);
    if (HSLP) __builtin_amdgcn_s_sleep(HSLP);
    u32x4 a0, a1;
    for (unsigned sp = 0; sp < SPIN_MAX; ++sp) {
      a0 = bload<AUX_SC1>(rws, vhh + w * 128 + kq * 16, 0); a1 = bload<AUX_SC1>(rws, vhh + w * 128 + 64 + kq * 16, 0);
      asm volatile("" ::: "memory");
      if (__ballot(!(full4(a0) & full4(a1))) == 0ull) break;
    }
    STAMP(1);
    f32x4 C[4];
#pragma unroll
    for (int t = 0; t < 4; ++t) C[t] = mfma(a0, GU[0][t], f32x4{0.f, 0.f, 0.f, 0.f});
#pragma unroll
    for (int t = 0; t < 4; ++t) C[t] = mfma(a1, GU[1][t], C[t]);
    l_gu[w * 64 + lane] = row_pick(C[0][0], C[1][0], C[2][0], C[3][0], lane);
    STAMP(11);
    wg_barrier();
    STAMP(15);
    {
      const f32x2 st = *(LDS const f32x2*)(sm + L_ST);
      float pr[NW];
#pragma unroll
      for (int ww = 0; ww < NW; ++ww) pr[ww] = l_gu[ww * 64 + lane];
      const float sum = ((pr[0] + pr[1]) + (pr[2] + pr[3])) + ((pr[4] + pr[5]) + (pr[6] + pr[7]));
      const float val = rbf(fmaf(-st[0], csum, sum) * st[1]);
      const float oth = dpp<XOR8>(val);
      const float gv = (c & 8) ? oth : val, uv = (c & 8) ? val : oth;
      const float tt = gv * fmaf(0.044715f * gv, gv, 1.f);
      const float ge = gv * __builtin_amdgcn_rcpf(1.f + __builtin_amdgcn_exp2f(-2.885390081777927f * 0.7978845608028654f * tt));
      const float act = rbf(rbf(ge) * uv);
      const unsigned pk = cvtpk(act, dpp<XOR1>(act));
      const u32x4 A = {dppu<BCAST0>(pk), dppu<BCAST0 + 2>(pk), dppu<BCAST0 + 4>(pk), dppu<BCAST0 + 6>(pk)};
      f32x4 Y[4];
#pragma unroll
      for (int t = 0; t < 4; ++t) Y[t] = mfma(A, Dn[t], f32x4{0.f, 0.f, 0.f, 0.f});
      __builtin_amdgcn_raw_buffer_store_b32(canon(asu(row_pick(Y[0][0], Y[1][0], Y[2][0], Y[3][0], lane))), rws, WS_Y2 + vyo, 0, AUX_SC1);
    }
    STAMP(12);
  }
}

extern "C" void kernel_launch(void* const* d_in, const int* in_sizes, int n_in, void* d_out, int out_size, void* d_ws, size_t ws_size,
                              hipStream_t stream) {
  Params p;
  p.x = (const uint16_t*)d_in[0]; p.k = (const uint16_t*)d_in[1]; p.v = (const uint16_t*)d_in[2]; p.wq = (const uint16_t*)d_in[3];
  p.wo = (const uint16_t*)d_in[4]; p.wg = (const uint16_t*)d_in[6]; p.wu = (const uint16_t*)d_in[7]; p.wd = (const uint16_t*)d_in[8];
  p.out = (uint16_t*)d_out; p.ws = (unsigned char*)d_ws;
  fused_kernel<<<256, NTHR, 0, stream>>>(p);
}
```

```cpp
#include <hip/hip_runtime.h>
#include <hip/hip_bf16.h>
#include <stdint.h>

#define DIAG 0
#define SLP 12
#define KVAUX 2
#define LPER 7000
#define LDEL 100
#define LPEER 16
#define LGMAX 300
#define LCLAMP 3000
#define LWM 5
#define LSTR 4352
#define CSLP 8
#define LEVERY 1600
#define LEV0 600
#define LRD 250
#define CSTOP 3
#define ROT 1
#define QROT 1
#define EAUX 0
#define QAUX 0
#define RHN 16
#define WRD 31
#define PRIO 1
#define HSLP 12
#define Y1SLP 24
#define Y2SLP 20
#define Y2SLN 64
#define NP1 1
#define NP2 2
#define NP3 1
#define STAG1 0
#define STAG2 4
#define STAG3 0
#define SLOW3 6
#define HROT 1
#define X4 0
#define PD 2
#define BQ 3
#define THR1 16
#define THR3 64

constexpr int B = 4, D = 512, H = 32, SK = 8192, DH = 128, F = 1376;
constexpr int NW = 8, NTHR = NW * 64 + 64, QW = 4;
constexpr int KEYS_WG = SK / 2;
constexpr int TILE = 4096;
constexpr int NV = KEYS_WG / 16 / NW;
constexpr int NK = NV;
constexpr int NF = F / 32;

typedef unsigned u32x8 __attribute__((ext_vector_type(8)));
typedef unsigned u32x4 __attribute__((ext_vector_type(4)));
typedef unsigned u32x2 __attribute__((ext_vector_type(2)));
typedef float f32x4 __attribute__((ext_vector_type(4)));
typedef float f32x2 __attribute__((ext_vector_type(2)));
typedef __bf16 bf16x2 __attribute__((ext_vector_type(2)));
typedef __bf16 bf16x8 __attribute__((ext_vector_type(8)));
typedef short i16x4 __attribute__((ext_vector_type(4)));
typedef __amdgpu_buffer_rsrc_t rsrc_t;
#define LDS __attribute__((address_space(3)))

__device__ __forceinline__ float dot2(unsigned a, unsigned b, float acc) {
  return __builtin_amdgcn_fdot2_f32_bf16(__builtin_bit_cast(bf16x2, a), __builtin_bit_cast(bf16x2, b), acc, false);
}
__device__ __forceinline__ float asf(unsigned u) { return __builtin_bit_cast(float, u); }
__device__ __forceinline__ unsigned asu(float f) { return __builtin_bit_cast(unsigned, f); }
__device__ __forceinline__ float bflo(unsigned u) { return asf(u << 16); }
__device__ __forceinline__ float bfhi(unsigned u) { return asf(u & 0xffff0000u); }
__device__ __forceinline__ unsigned cvtpk(float lo, float hi) {
  return __builtin_bit_cast(unsigned, __builtin_convertvector(f32x2{lo, hi}, bf16x2));
}
__device__ __forceinline__ float rbf(float x) { return bflo(cvtpk(x, x)); }
template <int CTRL> __device__ __forceinline__ float dpp(float x) {
  return asf(__builtin_amdgcn_mov_dpp((int)asu(x), CTRL, 0xf, 0xf, true));
}
template <int CTRL> __device__ __forceinline__ unsigned dppu(unsigned x) { return (unsigned)__builtin_amdgcn_mov_dpp((int)x, CTRL, 0xf, 0xf, true); }
constexpr int XOR1 = 0xB1, XOR2 = 0x4E, XOR7 = 0x141, XOR8 = 0x128, ROR4 = 0x124, BCAST0 = 0x150;
__device__ __forceinline__ float tsum32(float a, float b) { auto s = __builtin_amdgcn_permlane32_swap(asu(a), asu(b), false, false); return asf(s[0]) + asf(s[1]); }
__device__ __forceinline__ float tsum16(float a, float b) { auto s = __builtin_amdgcn_permlane16_swap(asu(a), asu(b), false, false); return asf(s[0]) + asf(s[1]); }
__device__ __forceinline__ float xrow_max(float x) {
  auto s = __builtin_amdgcn_permlane16_swap(asu(x), asu(x), false, false);
  x = fmaxf(asf(s[0]), asf(s[1]));
  auto t = __builtin_amdgcn_permlane32_swap(asu(x), asu(x), false, false);
  return fmaxf(asf(t[0]), asf(t[1]));
}
__device__ __forceinline__ float xrow_sum(float x) { x = tsum16(x, x); return tsum32(x, x); }
__device__ __forceinline__ float row_sum16(float x) { x += dpp<XOR8>(x); x += dpp<XOR7>(x); x += dpp<XOR2>(x); x += dpp<XOR1>(x); return x; }
__device__ __forceinline__ float wave_sum(float x) { return xrow_sum(row_sum16(x)); }
__device__ __forceinline__ f32x4 mfma(const u32x4& a, const u32x4& b, const f32x4& c) {
  return __builtin_amdgcn_mfma_f32_16x16x32_bf16(__builtin_bit_cast(bf16x8, a), __builtin_bit_cast(bf16x8, b), c, 0, 0, 0);
}
__device__ __forceinline__ u32x4 tr_operand(LDS const unsigned char* p0, LDS const unsigned char* p1) {
  const u32x2 a = __builtin_bit_cast(u32x2, __builtin_amdgcn_ds_read_tr16_b64_v4i16((LDS i16x4*)p0));
  const u32x2 b = __builtin_bit_cast(u32x2, __builtin_amdgcn_ds_read_tr16_b64_v4i16((LDS i16x4*)p1));
  return u32x4{a[0], a[1], b[0], b[1]};
}
__device__ __forceinline__ float row_pick(float a0, float a1, float a2, float a3, int lane) {
  const float x = (lane & 16) ? a1 : a0, y = (lane & 16) ? a3 : a2;
  return (lane & 32) ? y : x;
}

__device__ __forceinline__ rsrc_t mkrsrc(const void* p, unsigned bytes) { return __builtin_amdgcn_make_buffer_rsrc((void*)p, 0, bytes, 0x00020000); }
constexpr int AUX_NT = 2, AUX_SC1 = 16;
template <int AUX> __device__ __forceinline__ u32x4 bload(rsrc_t r, int voff, int soff) { return __builtin_amdgcn_raw_buffer_load_b128(r, voff, soff, AUX); }
template <int AUX> __device__ __forceinline__ u32x2 bload8(rsrc_t r, int voff, int soff) { return __builtin_amdgcn_raw_buffer_load_b64(r, voff, soff, AUX); }
__device__ __forceinline__ void sb() { __builtin_amdgcn_sched_barrier(0); }
__device__ __forceinline__ void wg_barrier() { asm volatile("s_waitcnt lgkmcnt(0)\n\ts_barrier" ::: "memory"); }

constexpr int RH = RHN;
constexpr int WS_EPOCH = 0;
constexpr int WS_CLR = 1024;
constexpr int WS_ML = 4096;
constexpr int WS_HH = 8192;
constexpr int WS_Y1 = WS_HH + B * RH * 1024;
constexpr int WS_Y2 = WS_Y1 + B * 64 * 2048;
constexpr int WS_LAG = (WS_Y2 + B * 64 * 2048 + 4095) & ~4095;
constexpr int WS_END = WS_LAG + 256 * (LSTR > 8 ? LSTR : 8);
constexpr unsigned EMPTY = 0xFFFFFFFFu;
constexpr unsigned SPIN_MAX = 1u << 18;
__device__ __forceinline__ unsigned canon(unsigned u) { return u < 0xFFFFFFFEu ? u : 0xFFFFFFFEu; }
__device__ __forceinline__ bool full4(const u32x4& v) { return (v[0] != EMPTY) & (v[1] != EMPTY) & (v[2] != EMPTY) & (v[3] != EMPTY); }

struct PollRegs { u32x8 j0, j1, j2, j3; };
#define P_S_(x) #x
#define P_S(x) P_S_(x)
#define P_V(b, i) "v[" P_S(b) "+" #i "]"
#define P_Q(b, i) "v[" P_S(b) "+" #i ":" P_S(b) "+" #i "+3]"
#define P_LD1(b) "buffer_load_dwordx4 " P_Q(b, 0) ", %[t0], %[rs], 0 offen offset:(" P_S(b) "-96)/8*%[so] sc1\n\t"
#define P_LDA(b) "buffer_load_dwordx4 " P_Q(b, 0) ", %[t0], %[rs], 0 offen sc1\n\t"
#define P_LD2(b) P_LDA(b) "buffer_load_dwordx4 " P_Q(b, 4) ", %[t1], %[rs], 0 offen sc1\n\t"
#define P_MV(b, i) "v_cndmask_b32 " P_V(128, i) ", " P_V(128, i) ", " P_V(b, i) ", vcc\n\t"
#define P_TAKE "s_and_b64 vcc, vcc, %[pend]\n\ts_andn2_b64 %[pend], %[pend], vcc\n\t"
#define P_PAUSE(k) "s_bcnt1_i32_b64 %[tmp], %[pend]\n\ts_cmp_le_u32 %[tmp], %[thr]\n\ts_cbranch_scc1 Lpf" #k "_%=\n\ts_sleep %[slow]\n\tLpf" #k "_%=:\n\ts_sleep %[fast]\n\t"
#define P_CHECK1(b, k, W) "s_waitcnt vmcnt(" #W ")\n\t" \
  "v_max3_u32 %[t0], " P_V(b, 0) ", " P_V(b, 1) ", " P_V(b, 2) "\n\tv_max_u32 %[t0], %[t0], " P_V(b, 3) "\n\t" \
  "v_cmp_ne_u32 vcc, -1, %[t0]\n\t" P_TAKE P_MV(b, 0) P_MV(b, 1) P_MV(b, 2) P_MV(b, 3) "s_cbranch_scc0 Lpd_%=\n\t" \
  "s_mov_b64 vcc, %[pend]\n\tv_cndmask_b32 %[t0], %[ve], %[va], vcc\n\t" P_LD1(b) P_PAUSE(k)
#define P_CHECK2(b, k, W) "s_waitcnt vmcnt(" #W ")\n\t" \
  "v_max3_u32 %[t0], " P_V(b, 0) ", " P_V(b, 1) ", " P_V(b, 2) "\n\tv_max3_u32 %[t1], " P_V(b, 3) ", " P_V(b, 4) ", " P_V(b, 5) "\n\t" \
  "v_max3_u32 %[t0], %[t0], " P_V(b, 6) ", " P_V(b, 7) "\n\tv_max_u32 %[t0], %[t0], %[t1]\n\t" \
  "v_cmp_ne_u32 vcc, -1, %[t0]\n\t" P_TAKE P_MV(b, 0) P_MV(b, 1) P_MV(b, 2) P_MV(b, 3) P_MV(b, 4) P_MV(b, 5) P_MV(b, 6) P_MV(b, 7) "s_cbranch_scc0 Lpd_%=\n\t" \
  "s_mov_b64 vcc, %[pend]\n\tv_cndmask_b32 %[t0], %[ve], %[va], vcc\n\tv_cndmask_b32 %[t1], %[ve], %[vb], vcc\n\t" P_LD2(b) P_PAUSE(k)
#define P_HEAD "s_nop 4\n\ts_mov_b64 %[pend], -1\n\ts_mov_b32 %[cnt], %[spin]\n\tv_mov_b32 %[t0], %[va]\n\tv_mov_b32 %[t1], %[vb]\n\t"
#define P_GAP "s_sleep %[fast]\n\t"
#define P_TAIL "s_sub_u32 %[cnt], %[cnt], 1\n\ts_cbranch_scc0 Lpl_%=\n\tLpd_%=:"
#define P_ASM(FIRST, LOOP) \
  asm volatile(P_HEAD FIRST "Lpl_%=:\n\t" LOOP P_TAIL \
               : "+{v[96:103]}"(J.j0), "+{v[104:111]}"(J.j1), "+{v[112:119]}"(J.j2), "+{v[120:127]}"(J.j3), "=&{v[128:135]}"(res), \
                 [t0] "=&v"(t0), [t1] "=&v"(t1), [pend] "=&s"(pend), [tmp] "=&s"(tmp), [cnt] "=&s"(cnt) \
               : [va] "v"(va), [vb] "v"(vb), [ve] "v"(ve), [rs] "s"(rs), [thr] "n"(THR), [slow] "n"(SLOW), [fast] "n"(FAST), [spin] "n"(SPIN), [so] "n"(SO) \
               : "vcc", "scc", "memory")
template <int NL, int NP, int THR, int SLOW, int FAST, unsigned SPIN, int SO = 0>
__device__ __forceinline__ u32x8 poll(PollRegs& J, const u32x4& rs, int va, int vb, int ve) {
  u32x8 res;
  unsigned t0, t1, tmp, cnt;
  unsigned long long pend;
  static_assert(NP >= 1 && NP <= 4 && (NL == 1 || NL == 2), "poll");
  if constexpr (NL == 1 && NP == 1) P_ASM(P_LD1(96) P_GAP, P_CHECK1(96, 0, 0));
  if constexpr (NL == 1 && NP == 2) P_ASM(P_LD1(96) P_GAP P_LD1(104) P_GAP, P_CHECK1(96, 0, 1) P_CHECK1(104, 1, 1));
  if constexpr (NL == 1 && NP == 3) P_ASM(P_LD1(96) P_GAP P_LD1(104) P_GAP P_LD1(112) P_GAP, P_CHECK1(96, 0, 2) P_CHECK1(104, 1, 2) P_CHECK1(112, 2, 2));
  if constexpr (NL == 1 && NP == 4) P_ASM(P_LD1(96) P_GAP P_LD1(104) P_GAP P_LD1(112) P_GAP P_LD1(120) P_GAP, P_CHECK1(96, 0, 3) P_CHECK1(104, 1, 3) P_CHECK1(112, 2, 3) P_CHECK1(120, 3, 3));
  if constexpr (NL == 2 && NP == 1) P_ASM(P_LD2(96) P_GAP, P_CHECK2(96, 0, 0));
  if constexpr (NL == 2 && NP == 2) P_ASM(P_LD2(96) P_GAP P_LD2(104) P_GAP, P_CHECK2(96, 0, 2) P_CHECK2(104, 1, 2));
  if constexpr (NL == 2 && NP == 3) P_ASM(P_LD2(96) P_GAP P_LD2(104) P_GAP P_LD2(112) P_GAP, P_CHECK2(96, 0, 4) P_CHECK2(104, 1, 4) P_CHECK2(112, 2, 4));
  if constexpr (NL == 2 && NP == 4) P_ASM(P_LD2(96) P_GAP P_LD2(104) P_GAP P_LD2(112) P_GAP P_LD2(120) P_GAP, P_CHECK2(96, 0, 6) P_CHECK2(104, 1, 6) P_CHECK2(112, 2, 6) P_CHECK2(120, 3, 6));
  return res;
}

constexpr int RBS = 320;
constexpr int L_RB = 0;
constexpr int L_ML = 2560;
constexpr int L_ST = 2624;
constexpr int L_LAG = 2688;
constexpr int L_GU = 2816;
constexpr int L_CS = L_GU + 2048;
constexpr int L_H = L_CS + 2048;
constexpr int L_S = 8192;
constexpr int L_E = L_S + KEYS_WG * 4;
constexpr int L_END = L_E + NW * 16384;

template <bool V> struct BoolC { static constexpr bool value = V; };
struct Params {
  const uint16_t* x; const uint16_t* k; const uint16_t* v; const uint16_t* wq; const uint16_t* wo;
  const uint16_t* wg; const uint16_t* wu; const uint16_t* wd; uint16_t* out; unsigned char* ws;
};

#define STAMP(i) do {} while (0)
#define PROG(t) do {} while (0)

__device__ __forceinline__ float dots(const u32x4& K, const u32x4& q) {
  float d = dot2(K[0], q[0], 0.f);
  d = dot2(K[1], q[1], d); d = dot2(K[2], q[2], d); return dot2(K[3], q[3], d);
}
__device__ __forceinline__ float score(float x0, float x1, float x2, float x3, int c) {
  const float a0 = x0 + dpp<XOR8>(x0), a2 = x2 + dpp<XOR8>(x2), a1 = x1 + dpp<XOR8>(x1), a3 = x3 + dpp<XOR8>(x3);
  const float y0 = (c & 8) ? a2 : a0, y1 = (c & 8) ? a3 : a1;
  const float t1 = y0 + dpp<XOR7>(y0), t2 = y1 + dpp<XOR7>(y1);
  float s = (c & 4) ? t2 : t1;
  s += dpp<XOR2>(s); s += dpp<XOR1>(s);
  return rbf(rbf(s) * 0.088388347648318440f) * 1.4426950408889634f;
}
__device__ __forceinline__ void pv(const u32x4& V, float pr, f32x2 (&acc)[4]) {
#pragma unroll
  for (int k = 0; k < 4; ++k) acc[k] = __builtin_elementwise_fma(f32x2{pr, pr}, f32x2{bflo(V[k]), bfhi(V[k])}, acc[k]);
}
__device__ __forceinline__ void pin4(f32x2 (&a)[4]) { asm volatile("" : "+v"(a[0]), "+v"(a[1]), "+v"(a[2]), "+v"(a[3]) :: "memory"); }
template <int N> __device__ __forceinline__ void issue(u32x4 (&T)[N], rsrc_t r, int voff, int soff) {
#pragma unroll
  for (int i = 0; i < N; ++i) T[i] = bload<KVAUX>(r, voff + i * 1024, soff);
  sb();
}
__device__ __forceinline__ void pair_rows(const u32x4& A, const u32x4& Bv, unsigned (&o)[8]) {
#pragma unroll
  for (int k = 0; k < 4; ++k) { o[2 * k] = __builtin_amdgcn_perm(Bv[k], A[k], 0x05040100u); o[2 * k + 1] = __builtin_amdgcn_perm(Bv[k], A[k], 0x07060302u); }
}
__device__ __forceinline__ float reduce_par(float t0, float t1, float t2, float t3) {
  float v = tsum16(tsum32(t0, t2), tsum32(t1, t3));
  v += dpp<XOR8>(v); v += dpp<ROR4>(v); v += dpp<XOR2>(v);
  return v;
}
__device__ __forceinline__ unsigned pack_rows(float x) { auto s = __builtin_amdgcn_permlane16_swap(asu(x), asu(x), false, false); return cvtpk(asf(s[0]), asf(s[1])); }
__device__ __forceinline__ void ln_stats(const float (&xs)[8], float& mean, float& rden) {
  float sum = 0.f;
#pragma unroll
  for (int k = 0; k < 8; ++k) sum += xs[k];
  const float mu = wave_sum(sum) * (1.f / D);
  float sq = 0.f;
#pragma unroll
  for (int k = 0; k < 8; ++k) { const float d = xs[k] - mu; sq = fmaf(d, d, sq); }
  const float var = wave_sum(sq) * (1.f / D);
  mean = rbf(mu);
  rden = rbf(sqrtf(rbf(rbf(var) + 1e-5f)));
}
__device__ __forceinline__ float max8(float m) { m = fmaxf(m, dpp<XOR7>(m)); m = fmaxf(m, dpp<XOR2>(m)); return fmaxf(m, dpp<XOR1>(m)); }

__global__ void __launch_bounds__(NTHR, 2) fused_kernel(Params p) {
  __shared__ __attribute__((aligned(1024))) unsigned char smem[L_END];
  LDS unsigned char* const sm = (LDS unsigned char*)smem;
  LDS float* const l_q = (LDS float*)(sm + L_RB);
  LDS u32x2* const l_ml = (LDS u32x2*)(sm + L_ML);
  LDS unsigned* const l_lag = (LDS unsigned*)(sm + L_LAG);
  LDS float* const l_gu = (LDS float*)(sm + L_GU);
  LDS float* const l_cs = (LDS float*)(sm + L_CS);
  LDS float* const l_s = (LDS float*)(sm + L_S);
  const int tid = threadIdx.x, lane = tid & 63, w = __builtin_amdgcn_readfirstlane(tid >> 6), r = lane >> 4, c = lane & 15;
  const int blk = blockIdx.x;
  const int h = 4 * (blk & 7) + (blk >> 6), b = (blk >> 3) & 3, s = (blk >> 5) & 1;
  const int idx = 2 * h + s;
  const unsigned t_start = (unsigned)__builtin_amdgcn_s_memrealtime();
  STAMP(0);
  asm volatile("" :: "s"(p.x), "s"(p.k), "s"(p.v), "s"(p.wq), "s"(p.wo), "s"(p.wg), "s"(p.wu), "s"(p.wd), "s"(p.out), "s"(p.ws));
  constexpr unsigned LP = LPER / (NK + NV);
  unsigned sched = LP;
  const size_t kvoff = ((size_t)(b * H + h) * SK + (size_t)s * KEYS_WG) * DH;
  const rsrc_t rk = mkrsrc(p.k + kvoff, KEYS_WG * DH * 2), rv = mkrsrc(p.v + kvoff, KEYS_WG * DH * 2);
  const rsrc_t rq = mkrsrc(p.wq + h * DH, D * H * DH * 2 - h * DH * 2);
  const rsrc_t rws = mkrsrc(p.ws, WS_END);
  const int j = b * 64 + idx;
  const bool ffn = idx < NF;
  const int vhh = WS_HH + (b * RH + (HROT ? (idx & (RH / NP2 - 1)) * NP2 : (idx & (RH - 1)))) * 1024;
  if (w == NW) {
    if (lane < 32) l_lag[lane] = (lane == 9 || lane >= 16) ? 0u : t_start - 100000u;
    if (lane < NW) l_ml[lane] = u32x2{EMPTY, EMPTY};
    const u32x4 e4 = {EMPTY, EMPTY, EMPTY, EMPTY};
#pragma unroll
    for (int i = 0; i < 2; ++i) __builtin_amdgcn_raw_buffer_store_b128(e4, rws, WS_Y1 + j * 2048 + i * 1024 + lane * 16, 0, AUX_SC1);
#pragma unroll
    for (int i = 0; i < 2; ++i) __builtin_amdgcn_raw_buffer_store_b128(e4, rws, WS_Y2 + j * 2048 + i * 1024 + lane * 16, 0, AUX_SC1);
    __builtin_amdgcn_raw_buffer_store_b128(e4, rws, lane < RH ? WS_HH + (b * RH + lane) * 1024 + idx * 16 : lane == RH ? WS_ML + j * 16 : WS_END, 0, AUX_SC1);
    sb();
    u32x2 er = bload8<AUX_SC1>(rws, WS_EPOCH + b * 256, 0);
    unsigned xres = p.x[b * D + 8 * idx + 4 * (c & 1) + r];
    sb();
    if (BQ > -2) asm volatile("s_barrier" ::: "memory");
    wg_barrier();
    asm volatile("s_waitcnt vmcnt(0)" : "+v"(er), "+v"(xres) :: "memory");
    const unsigned long long tg = ((unsigned long long)er[0] | ((unsigned long long)er[1] << 32)) + 1ull;
    const u32x2 tg2 = {(unsigned)tg, (unsigned)(tg >> 32)};
    __builtin_amdgcn_raw_buffer_store_b64(tg2, rws, lane == 0 ? WS_CLR + j * 8 : WS_END, 0, AUX_SC1);
    bool cok = false, mlp = false;
    auto publish_ml = [&]() {
      const u32x2 ml = l_ml[lane & 7];
      if (__ballot((ml[0] == EMPTY) | (ml[1] == EMPTY)) == 0ull) {
        const float M = max8(asf(ml[0]));
        float L = __builtin_amdgcn_exp2f(asf(ml[0]) - M) * asf(ml[1]);
        L += dpp<XOR7>(L); L += dpp<XOR2>(L); L += dpp<XOR1>(L);
        __builtin_amdgcn_raw_buffer_store_b64(u32x2{canon(asu(M)), canon(asu(L))}, rws, lane == 0 ? WS_ML + j * 16 : WS_END, 0, AUX_SC1);
        mlp = true;
      }
    };
    {
      const int lag_st = lane == 0 ? WS_LAG + blk * LSTR : WS_END;
      const int lag_ld = lane < LPEER ? WS_LAG + ((blk / LPEER) * LPEER + lane) * LSTR : WS_END;
      unsigned next = t_start + LEV0 + ((blk / LPEER) * LEVERY) / (256 / LPEER), gref = t_start - 100000u;
      unsigned nextr = next + LRD;
#pragma unroll 1
      for (int it = 0; it < 4096; ++it) {
        const unsigned stop = l_lag[9];
        const unsigned mine = l_lag[lane & 7], ref0 = __builtin_amdgcn_readfirstlane(mine);
        int d = (int)(mine - ref0);
        d += (int)dppu<XOR7>((unsigned)d); d += (int)dppu<XOR2>((unsigned)d); d += (int)dppu<XOR1>((unsigned)d); d >>= 3;
        const unsigned own = ref0 + (unsigned)d;
        const unsigned tnow = (unsigned)__builtin_amdgcn_s_memrealtime();
        if ((int)(tnow - next) >= 0) {
          next += LEVERY;
          __builtin_amdgcn_raw_buffer_store_b64(u32x2{own, ~own}, rws, lag_st, 0, AUX_SC1);
        }
        if ((int)(tnow - nextr) >= 0) {
          nextr += LEVERY;
          const u32x2 cm = bload8<AUX_SC1>(rws, lag_ld, 0);
          if (!cok) {
            const u32x2 cf = bload8<AUX_SC1>(rws, WS_CLR + (b * 64 + lane) * 8, 0);
            cok = __ballot((cf[0] != tg2[0]) | (cf[1] != tg2[1])) == 0ull;
          }
          int e = (int)(cm[0] - own);
          e = (cm[0] == ~cm[1] && e < LCLAMP) ? e : 0;
          e = max(e, (int)dppu<XOR8>((unsigned)e)); e = max(e, (int)dppu<XOR7>((unsigned)e)); e = max(e, (int)dppu<XOR2>((unsigned)e)); e = max(e, (int)dppu<XOR1>((unsigned)e));
          if constexpr (LPEER >= 32) { auto sx = __builtin_amdgcn_permlane16_swap((unsigned)e, (unsigned)e, false, false); e = max((int)sx[0], (int)sx[1]); }
          if constexpr (LPEER >= 64) { auto sx = __builtin_amdgcn_permlane32_swap((unsigned)e, (unsigned)e, false, false); e = max((int)sx[0], (int)sx[1]); }
          gref = own + (unsigned)e;
        }
        const int e2 = max((int)(__builtin_amdgcn_readfirstlane(gref) - own), 0);
        if (lane == 0) l_lag[8] = own + (unsigned)e2;
        if (!mlp) publish_ml();
        if (__builtin_amdgcn_readfirstlane(stop) != 0u) break;
        __builtin_amdgcn_s_sleep(CSLP);
        asm volatile("" ::: "memory");
      }
    }
    for (unsigned sp = 0; sp < SPIN_MAX && !mlp; ++sp) { publish_ml(); asm volatile("" ::: "memory"); }
    for (unsigned sp = 0; sp < SPIN_MAX && !cok; ++sp) {
      const u32x2 cf = bload8<AUX_SC1>(rws, WS_CLR + (b * 64 + lane) * 8, 0);
      cok = __ballot((cf[0] != tg2[0]) | (cf[1] != tg2[1])) == 0ull;
      if (!cok) __builtin_amdgcn_s_sleep(8);
      asm volatile("" ::: "memory");
    }
    __builtin_amdgcn_raw_buffer_store_b64(tg2, rws, (idx == 0 && lane == 0) ? WS_EPOCH + b * 256 : WS_END, 0, AUX_SC1);
    float cf0, cf1;
    {
      u32x2 m0, m1;
      for (unsigned sp = 0; sp < SPIN_MAX; ++sp) {
        m0 = bload8<AUX_SC1>(rws, WS_ML + (b * 64 + (lane >> 1)) * 16, 0);
        m1 = bload8<AUX_SC1>(rws, WS_ML + (b * 64 + 32 + (lane >> 1)) * 16, 0);
        asm volatile("" ::: "memory");
        if (__ballot((m0[0] == EMPTY) | (m0[1] == EMPTY) | (m1[0] == EMPTY) | (m1[1] == EMPTY)) == 0ull) break;
        __builtin_amdgcn_s_sleep(8);
      }
      const float a0 = asf(m0[0]), e0 = __builtin_amdgcn_exp2f(a0 - fmaxf(a0, dpp<XOR2>(a0))), l0 = asf(m0[1]) * e0;
      const float a1 = asf(m1[0]), e1 = __builtin_amdgcn_exp2f(a1 - fmaxf(a1, dpp<XOR2>(a1))), l1 = asf(m1[1]) * e1;
      cf0 = e0 / (l0 + dpp<XOR2>(l0)); cf1 = e1 / (l1 + dpp<XOR2>(l1));
      asm volatile("" : "+v"(cf0), "+v"(cf1) :: "memory");
    }
    if (PRIO) __builtin_amdgcn_s_setprio(3);
    const u32x4 rs4 = {(unsigned)(size_t)p.ws, (unsigned)((size_t)p.ws >> 32) & 0xFFFFu, (unsigned)WS_END, 0x00020000u};
    const rsrc_t rout = mkrsrc(p.out, B * D * 2);
    const int rep = (c >> 1) + 8 * (r & 1);
    int vend = WS_END, vy1 = WS_Y1 + j * 2048 + lane * 16, vy1b = vy1 + 1024, vy2 = WS_Y2 + j * 2048 + lane * 16;
    int vy2b = lane < 2 * (NF - 32) ? vy2 + 1024 : WS_END, vhl = vhh + lane * 16;
    int vhs = rep < RH ? WS_HH + (b * RH + rep) * 1024 + idx * 16 + (2 * (c & 1) + (r >> 1)) * 4 : WS_END;
    int vo = (lane & 0x1e) == 0 ? (b * D + idx * 8 + 4 * (c & 1) + (r & 2)) * 2 : B * D * 2;
    float xr = bflo(xres);
    asm volatile("" : "+v"(vend), "+v"(vy1), "+v"(vy1b), "+v"(vy2), "+v"(vy2b), "+v"(vhl), "+v"(vhs), "+v"(vo), "+v"(xr));
    PollRegs J;
    asm volatile("" : "={v[96:103]}"(J.j0), "={v[104:111]}"(J.j1), "={v[112:119]}"(J.j2), "={v[120:127]}"(J.j3));
    wg_barrier();
    STAMP(7);
    if (Y1SLP) __builtin_amdgcn_s_sleep(Y1SLP);
    float hres;
    {
      const u32x8 y = poll<2, NP1, THR1, SLP, STAG1, SPIN_MAX>(J, rs4, vy1, vy1b, vend);
      STAMP(9);
      const float sum = reduce_par(fmaf(cf1, asf(y[4]), cf0 * asf(y[0])), fmaf(cf1, asf(y[5]), cf0 * asf(y[1])),
                                   fmaf(cf1, asf(y[6]), cf0 * asf(y[2])), fmaf(cf1, asf(y[7]), cf0 * asf(y[3])));
      const unsigned pk = pack_rows(xr + rbf(sum));
      __builtin_amdgcn_raw_buffer_store_b32(canon(pk), rws, vhs, 0, AUX_SC1);
      sb();
      hres = (r & 1) ? bfhi(pk) : bflo(pk);
      STAMP(10);
    }
    if (ffn) {
      if (HSLP) __builtin_amdgcn_s_sleep(HSLP);
      const u32x8 y = poll<1, NP2, 64, 0, STAG2, SPIN_MAX, HROT * 1024>(J, rs4, vhl, vhl, vend);
      const u32x4 hv = {y[0], y[1], y[2], y[3]};
      *(LDS u32x4*)(sm + L_H + lane * 16) = hv;
      sb();
      STAMP(11);
      wg_barrier();
      sb();
      const float sh = bflo(__builtin_amdgcn_readfirstlane(hv[0]));
      f32x2 s1, s2;
#pragma unroll
      for (int k = 0; k < 4; ++k) {
        const f32x2 d = f32x2{bflo(hv[k]), bfhi(hv[k])} - f32x2{sh, sh};
        s1 = k ? s1 + d : d; s2 = k ? __builtin_elementwise_fma(d, d, s2) : d * d;
      }
      float sv = tsum32(s1[0] + s1[1], s2[0] + s2[1]);
      sv = row_sum16(tsum16(sv, sv));
      const float m1 = asf(__builtin_amdgcn_readlane(asu(sv), 0)) * (1.f / D), m2 = asf(__builtin_amdgcn_readlane(asu(sv), 32)) * (1.f / D);
      const float var = fmaxf(fmaf(-m1, m1, m2), 0.f);
      const float den = rbf(__builtin_amdgcn_sqrtf(rbf(rbf(var) + 1e-5f)));
      if (lane == 0) *(LDS f32x2*)(sm + L_ST) = f32x2{rbf(sh + m1), __builtin_amdgcn_rcpf(den)};
      STAMP(3);
      wg_barrier();
      if (Y2SLP) __builtin_amdgcn_s_sleep(Y2SLP);
    } else if (Y2SLN) {
      __builtin_amdgcn_s_sleep(Y2SLN);
    }
    {
      const u32x8 y = poll<2, NP3, THR3, SLOW3, STAG3, SPIN_MAX>(J, rs4, vy2, vy2b, vend);
      STAMP(13);
      const float sum = reduce_par(asf(y[0]) + asf(y[4]), asf(y[1]) + asf(y[5]), asf(y[2]) + asf(y[6]), asf(y[3]) + asf(y[7]));
      const unsigned pk = pack_rows(hres + rbf(sum));
      __builtin_amdgcn_raw_buffer_store_b32(pk, rout, vo, 0, 0);
      STAMP(14);
    }
    asm volatile("s_waitcnt vmcnt(0)" :: "{v[96:103]}"(J.j0), "{v[104:111]}"(J.j1), "{v[112:119]}"(J.j2), "{v[120:127]}"(J.j3) : "memory");
    return;
  }
  u32x4 X[2][2];
  const int voff = lane * 16;
  constexpr int tstride = NW * TILE;
  constexpr int WRAP = KEYS_WG * DH * 2 - 1;
  const int rot = ROT ? ((h & 7) * ROT + 8 * b + s + 2 * (h >> 3)) & 31 : 0;
  const int rbase = rot * tstride;
  int soff = (w * TILE + rbase) & WRAP;
  auto fill = [&]() { issue(X[0], rk, voff, soff); issue(X[1], rk, voff + 2048, soff); };
  u32x4 T[2 * PD - 1][4];
  if (w < 4) {
    const u32x4 xv = *reinterpret_cast<const u32x4*>(p.x + b * D + lane * 8);
    const int qr = QROT * 4 * b;
    const unsigned xo = *reinterpret_cast<const unsigned*>(p.x + b * D + 128 * w + 8 * ((c + qr) & 15) + 2 * r);
    sb();
    u32x4 W[32];
    const int vo = 2 * r * 8192 + c * 16, so = w * 128 * 8192;
#pragma unroll
    for (int i = 0; i < 32; ++i) W[i] = bload<QAUX>(rq, vo, so + (8 * (((i >> 1) + qr) & 15) + (i & 1)) * 8192);
    sb();
    if (BQ == -1) { asm volatile("s_barrier" ::: "memory"); sb(); }
    float xs[8];
#pragma unroll
    for (int k = 0; k < 4; ++k) { xs[2 * k] = bflo(xv[k]); xs[2 * k + 1] = bfhi(xv[k]); }
    float mean, den; ln_stats(xs, mean, den);
    unsigned xn = cvtpk(rbf(bflo(xo) - mean) / den, rbf(bfhi(xo) - mean) / den);
    asm volatile("" : "+v"(xn) :: "memory");
    sb();
    float a[8];
#pragma unroll
    for (int k = 0; k < 8; ++k) a[k] = 0.f;
#define QP(pp) { unsigned t[8]; pair_rows(W[2 * pp], W[2 * pp + 1], t); const unsigned xb = dppu<BCAST0 + pp>(xn); \
                 _Pragma("unroll") for (int e = 0; e < 8; ++e) a[e] = dot2(t[e], xb, a[e]); }
#define QS(pp) QP(pp) if (BQ == pp) { \
      asm volatile("" : "+v"(a[0]), "+v"(a[1]), "+v"(a[2]), "+v"(a[3]), "+v"(a[4]), "+v"(a[5]), "+v"(a[6]), "+v"(a[7]) :: "memory"); sb(); \
      asm volatile("s_barrier" ::: "memory"); sb(); }
    QS(0)
    QS(1) QS(2) QS(3) QS(4) QS(5) QS(6) QS(7)
    QS(8) QS(9) QS(10) QS(11) QS(12) QS(13) QS(14) QS(15)
#undef QS
#undef QP
    float u[4], v[2];
#pragma unroll
    for (int e = 0; e < 4; ++e) u[e] = tsum32(a[e], a[e + 4]);
#pragma unroll
    for (int e = 0; e < 2; ++e) v[e] = tsum16(u[e], u[e + 2]);
    *(LDS f32x2*)&l_q[w * DH + 8 * c + 4 * (lane >> 5) + 2 * (r & 1)] = f32x2{v[0], v[1]};
    X[0][0] = X[0][1] = X[1][0] = X[1][1] = u32x4{0u, 0u, 0u, 0u};
    asm volatile("" : "+v"(X[0][0]), "+v"(X[0][1]), "+v"(X[1][0]), "+v"(X[1][1]));
  } else {
    fill();
    if (BQ > -2) { asm volatile("s_barrier" ::: "memory"); sb(); }
#pragma unroll
    for (int i = 0; i < PD; ++i) {
      issue(T[PD - 1 + i], rk, voff, (soff - 4 * TILE + i * tstride) & WRAP);
      if (i + 1 < PD) issue(T[i], rk, voff, (soff + (i + 1) * tstride) & WRAP);
    }
  }
  wg_barrier();
  u32x4 q;
  {
    f32x4 a = {0, 0, 0, 0}, bq = {0, 0, 0, 0};
#pragma unroll
    for (int ww = 0; ww < QW; ++ww) {
      a += *(LDS const f32x4*)&l_q[ww * DH + 8 * c];
      bq += *(LDS const f32x4*)&l_q[ww * DH + 8 * c + 4];
    }
    q[0] = cvtpk(a[0], a[1]); q[1] = cvtpk(a[2], a[3]); q[2] = cvtpk(bq[0], bq[1]); q[3] = cvtpk(bq[2], bq[3]);
    asm volatile("" : "+v"(q[0]), "+v"(q[1]), "+v"(q[2]), "+v"(q[3]) :: "memory");
  }
  STAMP(4);
  soff = (soff + PD * tstride) & WRAP;
  auto gate = [&](unsigned lref) {
    const unsigned now = (unsigned)__builtin_amdgcn_s_memrealtime(), lag = now - sched;
    if (lane == 0) l_lag[w] = lag;
    const int wt = (int)(__builtin_amdgcn_readfirstlane(lref) - LDEL - lag);
    if (wt > 0) {
#pragma unroll 1
      for (int k = (min(wt, LGMAX) * LWM) >> 4; k > 0; --k) __builtin_amdgcn_s_sleep(1);
    }
    sched += LP;
    sb();
  };
  const int sl = w * 512 + r * 4 + (c >> 2);
  const unsigned gsz = ffn ? D * F * 2 - idx * 64 : 0, dsz = ffn ? 32 * D * 2 : 0;
  const int vgu = (8 * (lane >> 4) + ((lane >> 2) & 3)) * F * 2 + (lane & 3) * 16, sgu = w * 64 * F * 2;
  const int vd = (8 * (lane >> 5) + ((lane >> 3) & 3)) * 1024 + (lane & 7) * 16, sd = w * 128;
  LDS unsigned char* const eb = sm + L_E + w * 16384;
  LDS unsigned char* const park_gu = eb + lane * 16;
  LDS unsigned char* const park_d = eb + (lane >> 3) * 128 + (((lane & 7) ^ ((lane >> 3) & 6)) * 16);
  constexpr int NFE = 12, NE = NFE + 16;
  u32x4 E;
  auto eload = [&](int e) {
    const int i = e - NFE;
    const bool gu = e < 8;
    const uint16_t* base = i >= 0 ? p.wo + (size_t)h * DH * D : e < 4 ? p.wg + idx * 32 : e < 8 ? p.wu + idx * 32 : p.wd + (size_t)(ffn ? idx : 0) * 32 * D;
    const unsigned size = e >= NE ? 0u : i >= 0 ? DH * D * 2 : gu ? gsz : dsz;
    const int k = i >= 0 ? i : e & 3;
    const int so = gu ? sgu + (32 * (k >> 1) + 4 * (k & 1)) * F * 2 : sd + (32 * (k >> 2) + 16 * (k & 1) + 4 * ((k >> 1) & 1)) * 1024;
    E = bload<EAUX>(mkrsrc(base, size), gu ? vgu : vd, so);
    sb();
  };
  auto kround = [&](LDS float* dst, rsrc_t rn, int son, bool first_e = false) {
    const unsigned lref = l_lag[8];
    float x0 = dots(X[0][0], q), x1 = dots(X[0][1], q);
    asm volatile("" : "+v"(x0), "+v"(x1) :: "memory");
    sb();
    gate(lref);
    issue(X[0], rn, voff, son);
    if (first_e) eload(0);
    float x2 = dots(X[1][0], q), x3 = dots(X[1][1], q);
    asm volatile("" : "+v"(x2), "+v"(x3) :: "memory");
    sb();
    issue(X[1], rn, voff + 2048, son);
    *dst = score(x0, x1, x2, x3, c);
  };
  kround(w < 4 ? (LDS float*)(sm + L_E + w * 16384) + lane : l_s + sl, rk, soff);
  sched += (PD - 1) * LP;
  if (w >= 4) {
#pragma unroll
    for (int i = 0; i < 2 * PD - 1; ++i) {
      const int k = (i & 1) ? i >> 1 : PD - 1 + (i >> 1);
      const float x0 = dots(T[k][0], q), x1 = dots(T[k][1], q), x2 = dots(T[k][2], q), x3 = dots(T[k][3], q);
      l_s[(i & 1) ? sl + ((i >> 1) + 1) * 16 : sl - 4 * 512 + (i >> 1) * 16] = score(x0, x1, x2, x3, c);
      sb();
    }
    asm volatile("" ::: "memory");
    if (lane == 0) l_lag[12 + w] = 1u;
    asm volatile("" ::: "memory");
  }
  PROG(0);
#pragma unroll 1
  for (int t = PD; t < NK - 1; ++t) { soff = (soff + tstride) & WRAP; kround(l_s + sl + t * 16, rk, soff); PROG(t); }
  soff = w * TILE + rbase;
  kround(l_s + sl + (NV - 1) * 16, rv, soff, true);
  PROG(NV - 1);
  float mw;
  {
    if (w < 4) {
      for (unsigned sp = 0; sp < SPIN_MAX; ++sp) {
        asm volatile("" ::: "memory");
        if (__builtin_amdgcn_readfirstlane(l_lag[16 + w]) != 0u) break;
      }
    }
    asm volatile("" ::: "memory");
    f32x4 a = *(LDS const f32x4*)&l_s[w * 512 + lane * 8], bb = *(LDS const f32x4*)&l_s[w * 512 + lane * 8 + 4];
    float m = fmaxf(fmaxf(fmaxf(a[0], a[1]), fmaxf(a[2], a[3])), fmaxf(fmaxf(bb[0], bb[1]), fmaxf(bb[2], bb[3])));
    m = fmaxf(m, dpp<XOR8>(m)); m = fmaxf(m, dpp<XOR7>(m)); m = fmaxf(m, dpp<XOR2>(m)); m = fmaxf(m, dpp<XOR1>(m));
    mw = xrow_max(m);
#pragma unroll
    for (int e = 0; e < 4; ++e) { a[e] = __builtin_amdgcn_exp2f(a[e] - mw); bb[e] = __builtin_amdgcn_exp2f(bb[e] - mw); }
    *(LDS f32x4*)&l_s[w * 512 + lane * 8] = a; *(LDS f32x4*)&l_s[w * 512 + lane * 8 + 4] = bb;
    const float lw = wave_sum(((a[0] + a[1]) + (a[2] + a[3])) + ((bb[0] + bb[1]) + (bb[2] + bb[3])));
    if (lane == 0) l_ml[w] = u32x2{canon(asu(mw)), canon(asu(lw))};
    asm volatile("" : "+v"(mw) :: "memory");
  }
  f32x2 acc[4];
#pragma unroll
  for (int k = 0; k < 4; ++k) acc[k] = f32x2{0.f, 0.f};
  const int pl = w * 512 + r * 4;
  constexpr int NR = NV - 1;
  auto vround = [&](int t, auto parks) {
    constexpr bool PARK = decltype(parks)::value;
    soff = (soff + tstride) & WRAP;
    const unsigned lref = l_lag[8];
    const f32x4 p4 = *(LDS const f32x4*)&l_s[pl + t * 16];
    pv(X[0][0], p4[0], acc); pv(X[0][1], p4[1], acc);
    pin4(acc);
    if constexpr (PARK) *(LDS u32x4*)((t < 8 ? park_gu : park_d) + (t < NFE ? t : t - NFE) * 1024) = E;
    asm volatile("" ::: "memory");
    sb();
    gate(lref);
    issue(X[0], rv, voff, soff);
    if constexpr (PARK) eload(t + 1);
    pv(X[1][0], p4[2], acc); pv(X[1][1], p4[3], acc);
    pin4(acc);
    sb();
    issue(X[1], rv, voff + 2048, soff);
    if (t == NR - CSTOP && lane == 0) l_lag[9] = 1u;
    PROG(NV + t);
  };
#pragma unroll 1
  for (int t = 0; t < NFE; ++t) vround(t, BoolC<true>{});
  const int kq = r, tq = (lane >> 2) & 3, tp = lane & 3;
  const int g8 = 4 * (kq & 1) + tq;
  LDS const unsigned char* const tr_d = eb + (kq >> 1) * 1024 + g8 * 128 + (((tp >> 1) ^ (g8 & 6)) * 16) + (tp & 1) * 8;
  auto trd = [&](int t) { return (LDS const unsigned char*)(size_t)((unsigned)(size_t)tr_d ^ (unsigned)(t * 32)); };
  u32x4 GU[2][4], Dn[4];
  {
    LDS const unsigned char* const tr_g = eb + (tp >> 1) * 4096 + (4 * kq + tq) * 64 + (tp & 1) * 8;
#pragma unroll
    for (int ks = 0; ks < 2; ++ks)
#pragma unroll
      for (int t = 0; t < 4; ++t) GU[ks][t] = tr_operand(tr_g + 2 * ks * 1024 + t * 16, tr_g + (2 * ks + 1) * 1024 + t * 16);
#pragma unroll
    for (int t = 0; t < 4; ++t) Dn[t] = tr_operand(trd(t) + 8 * 1024, trd(t) + 10 * 1024);
    const u32x4 ones = {0x3F803F80u, 0x3F803F80u, 0x3F803F80u, 0x3F803F80u};
    f32x4 cs[4];
#pragma unroll
    for (int t = 0; t < 4; ++t) cs[t] = mfma(ones, GU[1][t], mfma(ones, GU[0][t], f32x4{0.f, 0.f, 0.f, 0.f}));
    l_cs[w * 64 + lane] = row_pick(cs[0][0], cs[1][0], cs[2][0], cs[3][0], lane);
#pragma unroll
    for (int t = 0; t < 4; ++t) asm volatile("" : "+v"(GU[0][t]), "+v"(GU[1][t]), "+v"(Dn[t]) :: "memory");
  }
#pragma unroll 1
  for (int t = NFE; t < NE; ++t) vround(t, BoolC<true>{});
  float sc;
  {
    u32x2 ml;
    for (unsigned sp = 0; sp < SPIN_MAX; ++sp) {
      ml = l_ml[lane & 7];
      asm volatile("" ::: "memory");
      if (__ballot((ml[0] == EMPTY) | (ml[1] == EMPTY)) == 0ull) break;
    }
    sc = __builtin_amdgcn_exp2f(mw - max8(asf(ml[0])));
    asm volatile("" : "+v"(sc) :: "memory");
  }
  u32x4 Wo[4][4];
  auto read_wo = [&]() {
#pragma unroll
    for (int ks = 0; ks < 4; ++ks)
#pragma unroll
      for (int t = 0; t < 4; ++t) Wo[ks][t] = tr_operand(trd(t) + 4 * ks * 1024, trd(t) + (4 * ks + 2) * 1024);
#pragma unroll
    for (int ks = 0; ks < 4; ++ks) asm volatile("" : "+v"(Wo[ks][0]), "+v"(Wo[ks][1]), "+v"(Wo[ks][2]), "+v"(Wo[ks][3]) :: "memory");
  };
#pragma unroll 1
  for (int t = NE; t < WRD; ++t) vround(t, BoolC<false>{});
  read_wo();
#pragma unroll 1
  for (int t = WRD; t < NR; ++t) vround(t, BoolC<false>{});
  {
    const f32x4 p4 = *(LDS const f32x4*)&l_s[pl + NR * 16];
    pv(X[0][0], p4[0], acc); pv(X[0][1], p4[1], acc);
    pin4(acc);
    sb();
    pv(X[1][0], p4[2], acc); pv(X[1][1], p4[3], acc);
    pin4(acc);
    sb();
  }
  PROG(NV + NR);
  STAMP(5);
  {
    float u[4], v[2];
#pragma unroll
    for (int e = 0; e < 4; ++e) u[e] = tsum32(acc[e >> 1][e & 1], acc[2 + (e >> 1)][e & 1]);
#pragma unroll
    for (int e = 0; e < 2; ++e) v[e] = tsum16(u[e], u[e + 2]);
    *(LDS unsigned*)(sm + L_RB + w * RBS + (4 * c + 2 * (lane >> 5) + (r & 1)) * 4) = cvtpk(v[0] * sc, v[1] * sc);
  }
  STAMP(6);
  wg_barrier();
  STAMP(7);
  const int vyo = WS_Y1 + (b * 64 + 8 * w + (lane >> 3)) * 2048 + idx * 32 + (lane & 7) * 4;
  auto put = [&](float v, int so) { __builtin_amdgcn_raw_buffer_store_b32(canon(asu(v)), rws, vyo, so, AUX_SC1); };
  {
    u32x4 A[4];
#pragma unroll
    for (int ks = 0; ks < 4; ++ks) A[ks] = *(LDS const u32x4*)(sm + L_RB + (lane & 7) * RBS + kq * 16 + ks * 64);
    sb();
    f32x4 C[4];
#pragma unroll
    for (int t = 0; t < 4; ++t) C[t] = mfma(A[0], Wo[0][t], f32x4{0.f, 0.f, 0.f, 0.f});
#pragma unroll
    for (int ks = 1; ks < 4; ++ks)
#pragma unroll
      for (int t = 0; t < 4; ++t) C[t] = mfma(A[ks], Wo[ks][t], C[t]);
    float sv[4];
#pragma unroll
    for (int t = 0; t < 4; ++t) { f32x2 x = f32x2{C[t][0], C[t][1]} + f32x2{C[t][2], C[t][3]}; asm volatile("" : "+v"(x)); sv[t] = x[0] + x[1]; }
    const float v01 = tsum16(sv[0], sv[1]), v23 = tsum16(sv[2], sv[3]);
    put((lane & 32) ? v23 : v01, 0);
  }
  STAMP(8);
  if (ffn) {
    float csum = 0.f;
#pragma unroll
    for (int ww = 0; ww < NW; ++ww) csum += l_cs[ww * 64 + lane];
    asm volatile("" : "+v"(csum) :: "memory");
    wg_barrier();
    STAMP(2);
    const u32x4 a0 = *(LDS const u32x4*)(sm + L_H + w * 128 + kq * 16), a1 = *(LDS const u32x4*)(sm + L_H + w * 128 + 64 + kq * 16);
    STAMP(1);
    f32x4 C[4];
#pragma unroll
    for (int t = 0; t < 4; ++t) C[t] = mfma(a0, GU[0][t], f32x4{0.f, 0.f, 0.f, 0.f});
#pragma unroll
    for (int t = 0; t < 4; ++t) C[t] = mfma(a1, GU[1][t], C[t]);
    l_gu[w * 64 + lane] = row_pick(C[0][0], C[1][0], C[2][0], C[3][0], lane);
    STAMP(11);
    wg_barrier();
    STAMP(15);
    {
      const f32x2 st = *(LDS const f32x2*)(sm + L_ST);
      float pr[NW];
#pragma unroll
      for (int ww = 0; ww < NW; ++ww) pr[ww] = l_gu[ww * 64 + lane];
      const float sum = ((pr[0] + pr[1]) + (pr[2] + pr[3])) + ((pr[4] + pr[5]) + (pr[6] + pr[7]));
      const float val = rbf(fmaf(-st[0], csum, sum) * st[1]);
      const float oth = dpp<XOR8>(val);
      const float gv = (c & 8) ? oth : val, uv = (c & 8) ? val : oth;
      const float tt = gv * fmaf(0.044715f * gv, gv, 1.f);
      const float ge = gv * __builtin_amdgcn_rcpf(1.f + __builtin_amdgcn_exp2f(-2.885390081777927f * 0.7978845608028654f * tt));
      const float act = rbf(rbf(ge) * uv);
      const unsigned pk = cvtpk(act, dpp<XOR1>(act));
      const u32x4 A = {dppu<BCAST0>(pk), dppu<BCAST0 + 2>(pk), dppu<BCAST0 + 4>(pk), dppu<BCAST0 + 6>(pk)};
      f32x4 Y[4];
#pragma unroll
      for (int t = 0; t < 4; ++t) Y[t] = mfma(A, Dn[t], f32x4{0.f, 0.f, 0.f, 0.f});
      put(row_pick(Y[0][0], Y[1][0], Y[2][0], Y[3][0], lane), WS_Y2 - WS_Y1);
    }
    STAMP(12);
  }
}

extern "C" void kernel_launch(void* const* d_in, const int* in_sizes, int n_in, void* d_out, int out_size, void* d_ws, size_t ws_size,
                              hipStream_t stream) {
  Params p;
  p.x = (const uint16_t*)d_in[0]; p.k = (const uint16_t*)d_in[1]; p.v = (const uint16_t*)d_in[2]; p.wq = (const uint16_t*)d_in[3];
  p.wo = (const uint16_t*)d_in[4]; p.wg = (const uint16_t*)d_in[6]; p.wu = (const uint16_t*)d_in[7]; p.wd = (const uint16_t*)d_in[8];
  p.out = (uint16_t*)d_out; p.ws = (unsigned char*)d_ws;
  fused_kernel<<<256, NTHR, 0, stream>>>(p);
}
```
